```python
import torch

def _softmax(x, axis=-1):
    return torch.softmax(x, dim=axis)

def _gelu(x):
    return torch.nn.functional.gelu(x, approximate='tanh')

def _silu(x):
    return torch.nn.functional.silu(x)

def _sigmoid(x):
    return torch.sigmoid(x)

def _layernorm(x, axis=-1, eps=1e-05):
    m = x.mean(dim=axis, keepdim=True)
    v = x.var(dim=axis, keepdim=True, unbiased=False)
    return (x - m) / torch.sqrt(v + eps)

def _scatter_add(tgt, idx, src, axis=0):
    return tgt.clone().index_add_(axis, idx, src)

def _segment_sum(src, seg, n):
    out = torch.zeros((n,) + src.shape[1:], dtype=src.dtype)
    return out.index_add_(0, seg, src)

def _pad(x, pw):
    flat = tuple((v for pair in reversed(pw) for v in pair))
    return torch.nn.functional.pad(x, flat)
kernel_name = 'adaLN_cond__linear_kernel_hedgehog__glu_gated_glu__prompt'

def setup_inputs():

    _w = lambda a: (lambda t: t.to(torch.bfloat16) if t.is_floating_point() else t)(torch.as_tensor(a))

    def _A_setup():
        B, S, D, H = (1, 2048, 512, 32)
        Dh = D // H
        return {'x': _w((lambda *s: torch.randn(*s, dtype=torch.bfloat16) * 0.05)(B, S, D)), 'wqkv': _w((lambda *s: torch.randn(*s, dtype=torch.bfloat16) * 0.4)(D, 3 * D)), 'w_phi': _w((lambda *s: torch.randn(*s, dtype=torch.bfloat16) * 0.4)(H, Dh, Dh) * Dh ** (-0.5)), 'wo': _w((lambda *s: torch.randn(*s, dtype=torch.bfloat16) * 0.05)(D, D))}
    _A_in = _A_setup()

    def _B_setup():
        B, S, D, F = (1, 2048, 512, 1376)
        return {'x': _w((lambda *s: torch.randn(*s, dtype=torch.bfloat16) * 0.05)(B, S, D)), 'w_gate': _w((lambda *s: torch.randn(*s, dtype=torch.bfloat16) * 0.1)(D, F)), 'w_up': _w((lambda *s: torch.randn(*s, dtype=torch.bfloat16) * 0.05)(D, F)), 'w_down': _w((lambda *s: torch.randn(*s, dtype=torch.bfloat16) * 0.05)(F, D))}
    _B_in = _B_setup()
    return {'A_x': _A_in['x'], 'A_wqkv': _A_in['wqkv'], 'A_w_phi': _A_in['w_phi'], 'A_wo': _A_in['wo'], 'B_x': _B_in['x'], 'B_w_gate': _B_in['w_gate'], 'B_w_up': _B_in['w_up'], 'B_w_down': _B_in['w_down'], 'c': _w((lambda *s: torch.randn(*s, dtype=torch.bfloat16) * 0.2)(1, 512) * 8.0), 'w_mod': _w((lambda *s: torch.randn(*s, dtype=torch.bfloat16) * 0.2)(512, 6 * 512) * 512 ** (-0.5) * 8.0)}

def reference(A_x, A_wqkv, A_w_phi, A_wo, B_x, B_w_gate, B_w_up, B_w_down, c, w_mod):

    def _A_ref(x, wqkv, w_phi, wo):
        B, S, D = x.shape
        H = 32
        Dh = D // H
        qkv = (lambda x, *d: x.permute(*d))((x @ wqkv).reshape(B, S, 3, H, Dh), 2, 0, 3, 1, 4)
        q, k, v = (qkv[0], qkv[1], qkv[2])
        pq = torch.einsum('bhsd,hdf->bhsf', q, w_phi)
        pk = torch.einsum('bhsd,hdf->bhsf', k, w_phi)
        phi_q = torch.concatenate([_softmax(pq, axis=-1), _softmax(-pq, axis=-1)], axis=-1)
        phi_k = torch.concatenate([_softmax(pk, axis=-1), _softmax(-pk, axis=-1)], axis=-1)
        kv = torch.einsum('bhsf,bhsd->bhfd', phi_k, v)
        z = torch.einsum('bhsf->bhf', phi_k)
        num = torch.einsum('bhqf,bhfd->bhqd', phi_q, kv)
        den = torch.einsum('bhqf,bhf->bhq', phi_q, z)[..., None] + 1e-06
        o = num / den
        return (lambda x, *d: x.permute(*d))(o, 0, 2, 1, 3).reshape(B, S, D) @ wo

    def _B_ref(x, w_gate, w_up, w_down):
        g = torch.einsum('bsd,df->bsf', x, w_gate)
        u = torch.einsum('bsd,df->bsf', x, w_up)
        h = _sigmoid(g) * u
        return torch.einsum('bsf,fd->bsd', h, w_down)
    x = A_x
    mod = (_silu(c) @ w_mod).reshape(c.shape[0], 6, 1, -1)
    sa, ca, ga, sb, cb, gb = (mod[:, 0].clone(), mod[:, 1].clone(), mod[:, 2].clone(), mod[:, 3].clone(), mod[:, 4].clone(), mod[:, 5].clone())
    h = _layernorm(x, axis=-1) * (1 + ca) + sa
    x = x + ga * _A_ref(h, A_wqkv, A_w_phi, A_wo)
    h = _layernorm(x, axis=-1) * (1 + cb) + sb
    x = x + gb * _B_ref(h, B_w_gate, B_w_up, B_w_down)
    return x
```

```cpp
#include <hip/hip_runtime.h>
#include <hip/hip_bf16.h>

typedef __bf16 bf16x8 __attribute__((ext_vector_type(8)));
typedef __bf16 bf16x2 __attribute__((ext_vector_type(2)));
typedef short s16x4 __attribute__((ext_vector_type(4)));
typedef float f32x16 __attribute__((ext_vector_type(16)));
typedef float f32x4 __attribute__((ext_vector_type(4)));
typedef float f32x2 __attribute__((ext_vector_type(2)));
typedef unsigned u32x4 __attribute__((ext_vector_type(4)));
typedef unsigned u32x2 __attribute__((ext_vector_type(2)));
typedef unsigned short u16;
#define LAS __attribute__((address_space(3)))
#define DEV __device__ __forceinline__

constexpr unsigned WS_EPOCH = 0;
constexpr unsigned WS_MODG = 4096;
constexpr unsigned WS_STG  = WS_MODG + 8192;
constexpr unsigned WS_UVG  = WS_STG + 32768;
constexpr unsigned WS_FLG  = WS_UVG + 45056;
constexpr unsigned FL_H = 0;
constexpr unsigned FL_O = FL_H + 32 * 32;
constexpr unsigned FL_X = FL_O + 64 * 16;
constexpr unsigned FL_D = FL_X + 64 * 16;
constexpr unsigned FL_N = FL_D + 64 * 48;
constexpr unsigned WS_KVP  = WS_FLG + FL_N * 64;
constexpr unsigned WS_ST2  = WS_KVP + 256 * 4352;
constexpr unsigned WS_H1   = WS_ST2 + 64 * 16 * 256;
constexpr unsigned WS_O    = WS_H1 + (2u << 20);
constexpr unsigned WS_XS   = WS_O + (2u << 20);
constexpr unsigned WS_HID  = WS_XS + (2u << 20);
constexpr unsigned WS_END  = WS_HID + 64 * 86 * 1024;
constexpr unsigned WS_DBG  = 32u << 20;
constexpr unsigned R0 = 0, R1 = 32768, R2 = 65536, R3 = 98304, R4 = 131072;
constexpr unsigned L_MOD = 155648;
constexpr unsigned L_UV = L_MOD + 4096;
constexpr unsigned L_RED = L_UV + 1536;
constexpr unsigned L_END = L_RED + 2048;
constexpr unsigned L_KVS = R4 + 18432, L_ZS = L_KVS + 2048;
static_assert(L_END <= 163840, "lds");

DEV float bflo(unsigned u) { return __uint_as_float(u << 16); }
DEV float bfhi(unsigned u) { return __uint_as_float(u & 0xffff0000u); }
DEV unsigned pk(float a, float b) { bf16x2 v = {(__bf16)a, (__bf16)b}; return __builtin_bit_cast(unsigned, v); }
DEV float rbf(float a) { return (float)(__bf16)a; }
DEV f32x16 mfma(u32x4 a, u32x4 b, f32x16 c) {
  return __builtin_amdgcn_mfma_f32_32x32x16_bf16(__builtin_bit_cast(bf16x8, a), __builtin_bit_cast(bf16x8, b), c, 0, 0, 0);
}
DEV u32x4 pk8(const f32x16& a, int o) {
  return u32x4{pk(a[o], a[o + 1]), pk(a[o + 2], a[o + 3]), pk(a[o + 4], a[o + 5]), pk(a[o + 6], a[o + 7])};
}
template <int ESTEP> DEV u32x4 wfrag(unsigned addr) {
  s16x4 a = __builtin_amdgcn_ds_read_tr16_b64_v4i16((LAS s16x4*)(size_t)addr);
  s16x4 b = __builtin_amdgcn_ds_read_tr16_b64_v4i16((LAS s16x4*)(size_t)(addr + ESTEP));
  u32x2 x = __builtin_bit_cast(u32x2, a), y = __builtin_bit_cast(u32x2, b);
  return u32x4{x[0], x[1], y[0], y[1]};
}
DEV unsigned opq(unsigned v) { asm volatile("" : "+v"(v)); return v; }
struct NoFill { DEV void operator()(int) const {} };
template <int N, int STEP, int ESTEP, int MODE, class F>
DEV void gemm2(unsigned ba, unsigned bb, const u32x4* af, f32x16& ca, f32x16& cb, F fill) {
  ba = opq(ba); bb = opq(bb);
  constexpr int D = MODE == 2 ? 4 : 2;
  u32x4 fa[D + 1], fb[D + 1];
#pragma unroll
  for (int s = 0; s < D; s++) { fa[s] = wfrag<ESTEP>(ba + s * STEP); if (MODE != 2) fb[s] = wfrag<ESTEP>(bb + s * STEP); }
#pragma unroll
  for (int s = 0; s < N; s++) {
    __builtin_amdgcn_sched_barrier(0);
    ca = mfma(fa[s % (D + 1)], af[s], ca);
    if (s + D < N) fa[(s + D) % (D + 1)] = wfrag<ESTEP>(ba + (s + D) * STEP);
    fill(2 * s);
    if (MODE != 2) {
      __builtin_amdgcn_sched_barrier(0);
      cb = MODE == 1 ? mfma(af[s], fb[s % (D + 1)], cb) : mfma(fb[s % (D + 1)], af[s], cb);
      if (s + D < N) fb[(s + D) % (D + 1)] = wfrag<ESTEP>(bb + (s + D) * STEP);
      fill(2 * s + 1);
    }
  }
  __builtin_amdgcn_sched_barrier(0);
}
template <int CTRL> DEV float dpp(float v) {
  return __int_as_float(__builtin_amdgcn_update_dpp(0, __float_as_int(v), CTRL, 0xf, 0xf, true));
}
DEV float sum32(float v) { u32x2 r = __builtin_amdgcn_permlane32_swap(__float_as_uint(v), __float_as_uint(v), false, false); return __uint_as_float(r[0]) + __uint_as_float(r[1]); }
DEV float sum16(float v) { u32x2 r = __builtin_amdgcn_permlane16_swap(__float_as_uint(v), __float_as_uint(v), false, false); return __uint_as_float(r[0]) + __uint_as_float(r[1]); }
DEV float max32(float v) { u32x2 r = __builtin_amdgcn_permlane32_swap(__float_as_uint(v), __float_as_uint(v), false, false); return fmaxf(__uint_as_float(r[0]), __uint_as_float(r[1])); }
DEV float wavesum(float v) { v += dpp<0xB1>(v); v += dpp<0x4E>(v); v += dpp<0x124>(v); v += dpp<0x128>(v); return sum32(sum16(v)); }
#define DMA1(LS) "s_nop 0\n\tglobal_load_lds_dwordx4 %1, %2\n\tv_add_u32 %1, %4, %1\n\ts_add_u32 m0, m0, " #LS "\n\t"
#define DMAN(BODY) unsigned keep; lds = __builtin_amdgcn_readfirstlane(lds); \
  asm volatile("s_mov_b32 %0, m0\n\ts_mov_b32 m0, %3\n\ts_nop 2\n\t" BODY "s_mov_b32 m0, %0" : "=&s"(keep), "+v"(voff) : "s"(sbase), "s"(lds), "i"(VSTEP) : "memory", "scc")
template <int VSTEP> DEV void dma8(const void* sbase, unsigned voff, unsigned lds) { DMAN(DMA1(1024) DMA1(1024) DMA1(1024) DMA1(1024) DMA1(1024) DMA1(1024) DMA1(1024) DMA1(1024)); }
template <int VSTEP> DEV void dma5(const void* sbase, unsigned voff, unsigned lds) { DMAN(DMA1(4096) DMA1(4096) DMA1(4096) DMA1(4096) DMA1(4096)); }
template <int VSTEP> DEV void dma6(const void* sbase, unsigned voff, unsigned lds) { DMAN(DMA1(4096) DMA1(4096) DMA1(4096) DMA1(4096) DMA1(4096) DMA1(4096)); }
template <int PITCH> DEV void dma_tile(const u16* src, int col, unsigned lds, int w, int l) {
  dma8<16 * PITCH>(src, (w * 128 + (l >> 2)) * PITCH + col * 2 + (l & 3) * 16, lds + w * 8192);
}
DEV int du_of(int m, int p) { return m < 11 ? 3 * m + p : 11 + 2 * m + p; }
template <int PART> DEV void dma_wd(const u16* src, int ct, int p, unsigned lds, int w, int l) {
  const unsigned voff = (du_of(PART == 0 ? 0 : PART == 1 ? 6 : 11, p) * 32 + (w & 1) * 16 + (l >> 2)) * 1024 + ct * 128 + (w >> 1) * 64 + (l & 3) * 16;
  if (PART == 0) dma6<3 * 32768>(src, voff, lds + w * 1024);
  else if (PART == 1) dma5<3 * 32768>(src, voff, lds + w * 1024);
  else dma5<2 * 32768>(src, voff, lds + w * 1024);
}
DEV void dma_wd16(const u16* src, int ct, int p, unsigned lds, int w, int l) {
  dma_wd<0>(src, ct, p, lds, w, l); dma_wd<1>(src, ct, p, lds + 6 * 4096, w, l); dma_wd<2>(src, ct, p, lds + 11 * 4096, w, l);
}
#define VMCNT(n) asm volatile("s_waitcnt vmcnt(" #n ")" ::: "memory")
#define BAR() asm volatile("s_waitcnt lgkmcnt(0)\n\ts_barrier" ::: "memory")
#define FENCE() asm volatile("" ::: "memory")
constexpr int SC1 = 16;
constexpr unsigned SPIN_MAX = 1u << 18;
#define TS(i)

DEV void wait_flags(__amdgpu_buffer_rsrc_t rs, unsigned first, int n, unsigned tag, int l, unsigned& budget) {
  unsigned off = WS_FLG + (first + (l < n ? l : 0)) * 64;
  FENCE();
  for (;;) {
    unsigned v = __builtin_amdgcn_raw_buffer_load_b32(rs, off, 0, SC1);
    if (__all(v == tag) || budget == 0) break;
    budget--;
    FENCE();
  }
  FENCE();
}
DEV void set_flag(__amdgpu_buffer_rsrc_t rs, unsigned idx, unsigned tag, int l) {
  if (l == 0) __builtin_amdgcn_raw_buffer_store_b32(tag, rs, WS_FLG + idx * 64, 0, SC1);
  FENCE();
}
DEV void gemv_red(float (&a)[8], unsigned col0, int t) {
#pragma unroll
  for (int j = 0; j < 8; j++) { a[j] += dpp<0x4E>(a[j]); a[j] += dpp<0x124>(a[j]); a[j] += dpp<0x128>(a[j]); }
  if ((t & 15) < 2) {
    LAS f32x4* p = (LAS f32x4*)(size_t)(L_RED + ((t >> 4) * 32 + col0 + (t & 1) * 8) * 4);
    p[0] = f32x4{a[0], a[1], a[2], a[3]}; p[1] = f32x4{a[4], a[5], a[6], a[7]};
  }
}
DEV void gemv_acc(float (&a)[8], u32x4 v, float s) {
#pragma unroll
  for (int j = 0; j < 4; j++) { a[2 * j] += s * bflo(v[j]); a[2 * j + 1] += s * bfhi(v[j]); }
}
DEV float red_col(int col, int half) {
  float s = 0;
#pragma unroll
  for (int r = 0; r < 8; r++) s += *(LAS float*)(size_t)(L_RED + ((half * 8 + r) * 32 + col) * 4);
  return sum32(s);
}
DEV void phi(const f32x16& x, int o, u32x4& pp, u32x4& pm) {
  float mx = x[o], mn = x[o];
#pragma unroll
  for (int j = 1; j < 8; j++) { mx = fmaxf(mx, x[o + j]); mn = fminf(mn, x[o + j]); }
  mx = max32(mx); mn = -max32(-mn);
  const float L = 1.44269504088896f;
  float ep[8], em[8], sp = 0, sm = 0, a = -mx * L, c = mn * L;
#pragma unroll
  for (int j = 0; j < 8; j++) {
    ep[j] = __builtin_amdgcn_exp2f(fmaf(x[o + j], L, a)); em[j] = __builtin_amdgcn_exp2f(fmaf(x[o + j], -L, c));
    sp += ep[j]; sm += em[j];
  }
  sp = __builtin_amdgcn_rcpf(sum32(sp)); sm = __builtin_amdgcn_rcpf(sum32(sm));
#pragma unroll
  for (int j = 0; j < 4; j++) { pp[j] = pk(ep[2 * j] * sp, ep[2 * j + 1] * sp); pm[j] = pk(em[2 * j] * sm, em[2 * j + 1] * sm); }
}

__global__ __launch_bounds__(256, 1) void fused_block(const u16* __restrict__ x, const u16* __restrict__ wqkv, const u16* __restrict__ wphi,
    const u16* __restrict__ wo, const u16* __restrict__ wg, const u16* __restrict__ wu, const u16* __restrict__ wd,
    const u16* __restrict__ cvec, const u16* __restrict__ wmod, u16* __restrict__ out, void* ws) {
  extern __shared__ __attribute__((aligned(16))) char smem[];
  const int t = threadIdx.x, l = t & 63, w = __builtin_amdgcn_readfirstlane(t >> 6), b = blockIdx.x;
  const int xc = b & 7, j = b >> 3, g = xc * 2 + (j >> 4), m = j & 15, h = l >> 5, r = l & 31;
  const int th = m >> 3, ct = m & 7, tt = w & 1, kh = w >> 1;
  const __amdgpu_buffer_rsrc_t rs = __builtin_amdgcn_make_buffer_rsrc(ws, 0, WS_END, 0x00020000);
  unsigned budget = SPIN_MAX;
  const f32x16 Z = {0, 0, 0, 0, 0, 0, 0, 0, 0, 0, 0, 0, 0, 0, 0, 0};
  TS(0); TS(1);
  const unsigned tagv = __builtin_amdgcn_raw_buffer_load_b32(rs, WS_EPOCH, 0, SC1);
  const int srow = xc * 256 + j * 8 + w * 2;
  const u32x4 xr0 = *(const u32x4*)(x + (size_t)srow * 512 + l * 8), xr1 = *(const u32x4*)(x + (size_t)(srow + 1) * 512 + l * 8);
  u16 cv[4]; u32x4 wa[4], wc[4], w2[4], xh[2];
  const int grow = t >> 1, gcol = (t & 1) * 8, i2 = (b & 7) * 16 + (j & 15);
#pragma unroll
  for (int i = 0; i < 4; i++) cv[i] = cvec[grow + 128 * i];
#pragma unroll
  for (int i = 0; i < 4; i++) {
    wa[i] = *(const u32x4*)(wmod + (size_t)(grow + 128 * i) * 3072 + j * 16 + gcol);
    wc[i] = *(const u32x4*)(wmod + (size_t)(grow + 128 * i) * 3072 + 512 + j * 16 + gcol);
  }
#pragma unroll
  for (int i = 0; i < 2; i++) xh[i] = *(const u32x4*)(x + (size_t)(xc * 256 + w * 64 + i * 32 + r) * 512 + j * 16 + h * 8);
  if (j < 16) {
#pragma unroll
    for (int i = 0; i < 4; i++) w2[i] = *(const u32x4*)(wmod + (size_t)(grow + 128 * i) * 3072 + 1024 + i2 * 16 + gcol);
  }
  const unsigned tag = __builtin_amdgcn_readfirstlane(tagv) + 1;
  {
    float v0[8], v1[8], s0 = 0, s1 = 0;
#pragma unroll
    for (int i = 0; i < 4; i++) { v0[2 * i] = bflo(xr0[i]); v0[2 * i + 1] = bfhi(xr0[i]); v1[2 * i] = bflo(xr1[i]); v1[2 * i + 1] = bfhi(xr1[i]); }
#pragma unroll
    for (int i = 0; i < 8; i++) { s0 += v0[i]; s1 += v1[i]; }
    s0 = wavesum(s0) * (1.f / 512); s1 = wavesum(s1) * (1.f / 512);
    float q0 = 0, q1 = 0;
#pragma unroll
    for (int i = 0; i < 8; i++) { q0 += (v0[i] - s0) * (v0[i] - s0); q1 += (v1[i] - s1) * (v1[i] - s1); }
    q0 = __builtin_amdgcn_rsqf(wavesum(q0) * (1.f / 512) + 1e-5f); q1 = __builtin_amdgcn_rsqf(wavesum(q1) * (1.f / 512) + 1e-5f);
    if (l < 2) __builtin_amdgcn_raw_buffer_store_b128(u32x4{__float_as_uint(l ? s1 : s0), tag, __float_as_uint(l ? q1 : q0), tag}, rs, WS_STG + (srow + l) * 16, 0, SC1);
  }
  TS(2);
  dma_tile<3072>(wqkv, 512 + m * 32, R0, w, l);
  dma_tile<3072>(wqkv, 1024 + m * 32, R1, w, l);
  float sc[4];
#pragma unroll
  for (int i = 0; i < 4; i++) { float c = bflo(cv[i]); sc[i] = rbf(c * __builtin_amdgcn_rcpf(1.f + __builtin_amdgcn_exp2f(-1.44269504088896f * c))); }
  {
    float a[8] = {0, 0, 0, 0, 0, 0, 0, 0}, c[8] = {0, 0, 0, 0, 0, 0, 0, 0};
#pragma unroll
    for (int i = 0; i < 4; i++) { gemv_acc(a, wa[i], sc[i]); gemv_acc(c, wc[i], sc[i]); }
    gemv_red(a, 0, t); gemv_red(c, 16, t);
  }
  BAR();
  TS(3);
  {
    float mv = rbf(red_col(r, h));
    if (r >= 16) mv = rbf(1.f + mv);
    float sa[8], c1[8];
#pragma unroll
    for (int i = 0; i < 8; i++) { sa[i] = __shfl(mv, h * 8 + i); c1[i] = __shfl(mv, 16 + h * 8 + i); }
    u32x4 st[2];
    FENCE();
    for (;;) {
#pragma unroll
      for (int i = 0; i < 2; i++) st[i] = __builtin_amdgcn_raw_buffer_load_b128(rs, WS_STG + (xc * 256 + w * 64 + i * 32 + r) * 16, 0, SC1);
      if (__all(st[0][1] == tag && st[0][3] == tag && st[1][1] == tag && st[1][3] == tag) || budget == 0) break;
      budget--;
      FENCE();
    }
    TS(4);
#pragma unroll
    for (int i = 0; i < 2; i++) {
      float mean = __uint_as_float(st[i][0]), rstd = __uint_as_float(st[i][2]);
      u32x4 o;
#pragma unroll
      for (int q = 0; q < 4; q++)
        o[q] = pk(fmaf((bflo(xh[i][q]) - mean) * rstd, c1[2 * q], sa[2 * q]), fmaf((bfhi(xh[i][q]) - mean) * rstd, c1[2 * q + 1], sa[2 * q + 1]));
      __builtin_amdgcn_raw_buffer_store_b128(o, rs, WS_H1 + ((xc * 8 + w * 2 + i) * 32 + j) * 1024 + l * 16, 0, SC1);
    }
    dma_tile<3072>(wqkv, m * 32, R2, w, l);
    VMCNT(8);
    set_flag(rs, FL_H + (xc * 4 + w) * 32 + j, tag, l);
  }
  TS(5);
  BAR();
  if (j < 16) {
    float a[8] = {0, 0, 0, 0, 0, 0, 0, 0};
#pragma unroll
    for (int i = 0; i < 4; i++) gemv_acc(a, w2[i], sc[i]);
    gemv_red(a, 0, t);
    BAR();
    if (w == 0) {
      float v = red_col(r & 15, h), v1 = dpp<0xB1>(v);
      if (l < 16 && !(l & 1)) __builtin_amdgcn_raw_buffer_store_b64(u32x2{pk(v, v1), tag}, rs, WS_MODG + (i2 * 8 + (l >> 1)) * 8, 0, SC1);
    }
  }
  u32x4 ph0, ph1, idp, idm;
  {
    unsigned e[8];
#pragma unroll
    for (int i = 0; i < 8; i++) e[i] = wphi[(2 * m + (r >> 4)) * 256 + (8 * (i >> 2) + 4 * h + (i & 3)) * 16 + (r & 15)];
#pragma unroll
    for (int i = 0; i < 4; i++) {
      unsigned v = e[2 * i] | (e[2 * i + 1] << 16);
      ph0[i] = r < 16 ? v : 0; ph1[i] = r < 16 ? 0 : v;
      int k0 = 8 * (i >> 1) + 4 * h + 2 * (i & 1);
      unsigned id = ((r & 15) == k0 ? 0x3f80u : 0) | ((r & 15) == k0 + 1 ? 0x3f800000u : 0);
      idp[i] = r < 16 ? id : 0; idm[i] = r < 16 ? 0 : id;
    }
  }
  const int tile = g * 4 + w;
  const int tile2 = g * 4 + th * 2 + tt;
  const int n0 = ct * 64 + kh * 32;
  const unsigned trb = ((l & 15) >> 2) * 64 + ((l >> 4) & 1) * 32 + (l & 3) * 8;
  const unsigned trn = trb + h * 512, trp = trb + h * 256;
  u32x4 af[32];
  wait_flags(rs, FL_H + (tile >> 1) * 32, 32, tag, l, budget);
  TS(6);
#pragma unroll
  for (int s = 0; s < 32; s++) af[s] = __builtin_amdgcn_raw_buffer_load_b128(rs, l * 16, WS_H1 + (tile * 32 + s) * 1024, SC1);
  const int iu = xc * 22 + j;
  const bool do_uv = j < 22 && iu < 172;
  u32x4 wuv[4]; u32x2 xres[4];
  {
    f32x16 ak = Z, av = Z;
    gemm2<32, 1024, 256, 1>(R0 + trn, R1 + trn, af, ak, av, NoFill());
    TS(7);
    f32x16 pkk = mfma(ph1, pk8(ak, 8), mfma(ph0, pk8(ak, 0), Z));
    u32x4 v0 = pk8(av, 0), v1 = pk8(av, 8);
    const u32x4 ones = {0x3f803f80u, 0x3f803f80u, 0x3f803f80u, 0x3f803f80u};
    u32x4 pp, pm;
    phi(pkk, 0, pp, pm);
    f32x16 tr = mfma(pm, idm, mfma(pp, idp, Z));
    f32x16 kv0 = mfma(pk8(tr, 8), r < 16 ? v1 : ones, mfma(pk8(tr, 0), r < 16 ? v0 : ones, Z));
    phi(pkk, 8, pp, pm);
    tr = mfma(pm, idm, mfma(pp, idp, Z));
    f32x16 kv1 = mfma(pk8(tr, 8), r < 16 ? ones : v1, mfma(pk8(tr, 0), r < 16 ? ones : v0, Z));
#pragma unroll
    for (int i = 0; i < 4; i++) {
      f32x4 a, z;
#pragma unroll
      for (int q = 0; q < 4; q++) { a[q] = r < 16 ? kv0[4 * i + q] : kv1[4 * i + q]; z[q] = r < 16 ? kv1[4 * i + q] : kv0[4 * i + q]; }
      *(LAS f32x4*)(size_t)(R4 + w * 4352 + i * 1024 + l * 16) = a;
      if ((l & 15) == 0) *(LAS f32x4*)(size_t)(R4 + w * 4352 + 4096 + ((l >> 4) * 4 + i) * 16) = z;
    }
  }
  VMCNT(0);
  BAR();
  {
    f32x4 a = {0, 0, 0, 0}, z = {0, 0, 0, 0};
#pragma unroll
    for (int i = 0; i < 4; i++) { a += *(LAS f32x4*)(size_t)(R4 + i * 4352 + t * 16); z += *(LAS f32x4*)(size_t)(R4 + i * 4352 + 4096 + (t & 15) * 16); }
    const unsigned base = WS_KVP + (m * 16 + g) * 4352;
    __builtin_amdgcn_raw_buffer_store_b128(u32x4{pk(a[0], a[1]), tag, pk(a[2], a[3]), tag}, rs, base + t * 16, 0, SC1);
    if (t < 16) __builtin_amdgcn_raw_buffer_store_b128(u32x4{pk(z[0], z[1]), tag, pk(z[2], z[3]), tag}, rs, base + 4096 + t * 16, 0, SC1);
  }
  TS(8);
  if (do_uv) {
    const u16* src = (iu < 86 ? wg + iu * 16 : wu + (iu - 86) * 16) + gcol;
#pragma unroll
    for (int i = 0; i < 4; i++) wuv[i] = *(const u32x4*)(src + (size_t)(grow + 128 * i) * 1376);
  }
#pragma unroll
  for (int i = 0; i < 4; i++) xres[i] = *(const u32x2*)(x + (size_t)(tile2 * 32 + r) * 512 + n0 + 8 * i + 4 * h);
  FENCE();
  dma_tile<1024>(wo, ct * 64, R0, w, l);
  dma_tile<1024>(wo, ct * 64 + 32, R1, w, l);
  dma_tile<2752>(wg, du_of(m, 0) * 32, R3, w, l);
  u32x4 qp0, qm0, qp1, qm1;
  {
    f32x16 aq = Z;
    gemm2<32, 1024, 256, 2>(R2 + trn, 0, af, aq, aq, NoFill());
    f32x16 pq = mfma(ph1, pk8(aq, 8), mfma(ph0, pk8(aq, 0), Z));
    phi(pq, 0, qp0, qm0); phi(pq, 8, qp1, qm1);
  }
  TS(9);
  {
    u32x4 a, c;
    FENCE();
    for (;;) {
      a = __builtin_amdgcn_raw_buffer_load_b128(rs, WS_MODG + t * 16, 0, SC1);
      c = __builtin_amdgcn_raw_buffer_load_b128(rs, WS_MODG + 4096 + t * 16, 0, SC1);
      if (__all(a[1] == tag && a[3] == tag && c[1] == tag && c[3] == tag) || budget == 0) break;
      budget--;
      FENCE();
    }
    if (t < 128) { c[0] = pk(1.f + bflo(c[0]), 1.f + bfhi(c[0])); c[2] = pk(1.f + bflo(c[2]), 1.f + bfhi(c[2])); }
    *(LAS u32x2*)(size_t)(L_MOD + t * 8) = u32x2{a[0], a[2]};
    *(LAS u32x2*)(size_t)(L_MOD + 2048 + t * 8) = u32x2{c[0], c[2]};
  }
  {
    u32x4 p[16], z;
    FENCE();
    for (;;) {
      bool ok = true;
#pragma unroll
      for (int i = 0; i < 16; i++) p[i] = __builtin_amdgcn_raw_buffer_load_b128(rs, t * 16, WS_KVP + (m * 16 + i) * 4352, SC1);
      z = __builtin_amdgcn_raw_buffer_load_b128(rs, (t >> 4) * 4352 + 4096 + (t & 15) * 16, WS_KVP + m * 16 * 4352, SC1);
#pragma unroll
      for (int i = 0; i < 16; i++) ok = ok && p[i][1] == tag && p[i][3] == tag;
      ok = ok && z[1] == tag && z[3] == tag;
      if (__all(ok) || budget == 0) break;
      budget--;
      FENCE();
    }
    TS(10);
    f32x4 a = {0, 0, 0, 0};
#pragma unroll
    for (int i = 0; i < 16; i++) a += f32x4{bflo(p[i][0]), bfhi(p[i][0]), bflo(p[i][2]), bfhi(p[i][2])};
    *(LAS u32x2*)(size_t)(L_KVS + l * 32 + w * 8) = u32x2{pk(a[0], a[1]), pk(a[2], a[3])};
    f32x4 zz = {sum32(sum16(bflo(z[0]))), sum32(sum16(bfhi(z[0]))), sum32(sum16(bflo(z[2]))), sum32(sum16(bfhi(z[2])))};
    if (l < 16) *(LAS f32x4*)(size_t)(L_ZS + w * 256 + l * 16) = zz;
  }
  BAR();
  {
    u32x4 k0 = *(LAS u32x4*)(size_t)(L_KVS + l * 32), k1 = *(LAS u32x4*)(size_t)(L_KVS + l * 32 + 16);
    f32x16 zf;
#pragma unroll
    for (int i = 0; i < 4; i++) {
      f32x4 z = {0, 0, 0, 0};
#pragma unroll
      for (int q = 0; q < 4; q++) z += *(LAS f32x4*)(size_t)(L_ZS + q * 256 + ((l >> 4) * 4 + i) * 16);
      zf[4 * i] = z[0]; zf[4 * i + 1] = z[1]; zf[4 * i + 2] = z[2]; zf[4 * i + 3] = z[3];
    }
    u32x4 z0 = pk8(zf, 0), z1 = pk8(zf, 8);
    f32x16 o0 = mfma(r < 16 ? k1 : z1, qm0, mfma(r < 16 ? k0 : z0, qp0, Z));
    f32x16 o1 = mfma(r < 16 ? z1 : k1, qm1, mfma(r < 16 ? z0 : k0, qp1, Z));
    float d0 = __builtin_amdgcn_rcpf(o0[8] + 1e-6f), d1 = __builtin_amdgcn_rcpf(o1[0] + 1e-6f);
    o0 *= d0; o1 *= d1;
    __builtin_amdgcn_raw_buffer_store_b128(pk8(o0, 0), rs, l * 16, WS_O + (tile * 32 + 2 * m) * 1024, SC1);
    __builtin_amdgcn_raw_buffer_store_b128(pk8(o1, 8), rs, l * 16, WS_O + (tile * 32 + 2 * m + 1) * 1024, SC1);
    dma_tile<2752>(wu, du_of(m, 0) * 32, R2, w, l);
    VMCNT(8);
    set_flag(rs, FL_O + tile * 16 + m, tag, l);
  }
  TS(11);
  if (do_uv) {
    float a[8] = {0, 0, 0, 0, 0, 0, 0, 0}, c[8] = {0, 0, 0, 0, 0, 0, 0, 0};
#pragma unroll
    for (int i = 0; i < 4; i++) {
      gemv_acc(a, wuv[i], bflo(*(LAS u16*)(size_t)(L_MOD + 2048 + (grow + 128 * i) * 2)));
      gemv_acc(c, wuv[i], bflo(*(LAS u16*)(size_t)(L_MOD + 1024 + (grow + 128 * i) * 2)));
    }
    gemv_red(a, 0, t); gemv_red(c, 16, t);
    BAR();
    if (w == 0) {
      float v = red_col(r, h);
      if (l < 32) __builtin_amdgcn_raw_buffer_store_b64(u32x2{__float_as_uint(v), tag}, rs, WS_UVG + (iu * 16 + (l & 15)) * 16 + (l >> 4) * 8, 0, SC1);
    }
  }
  float x1[16];
  wait_flags(rs, FL_O + tile2 * 16 + kh * 8, 8, tag, l, budget);
  TS(12);
  {
#pragma unroll
    for (int s = 0; s < 16; s++) af[s] = __builtin_amdgcn_raw_buffer_load_b128(rs, l * 16, WS_O + (tile2 * 32 + kh * 16 + s) * 1024, SC1);
    f32x16 a0 = Z, a1 = Z;
    const unsigned b0 = trp + kh * (16384 + 32768), b1 = trp + kh * 16384 + (1 - kh) * 32768;
    gemm2<16, 1024, 512, 0>(b1, b0, af, a1, a0, NoFill());
#pragma unroll
    for (int i = 0; i < 4; i++) *(LAS f32x4*)(size_t)(R4 + (w ^ 2) * 4096 + i * 1024 + l * 16) = f32x4{a1[4 * i], a1[4 * i + 1], a1[4 * i + 2], a1[4 * i + 3]};
    TS(13);
    BAR();
    float s1 = 0, s2 = 0; f32x16 xs;
#pragma unroll
    for (int i = 0; i < 4; i++) {
      f32x4 o = *(LAS f32x4*)(size_t)(R4 + w * 4096 + i * 1024 + l * 16);
      u32x2 ga = *(LAS u32x2*)(size_t)(L_MOD + (n0 + 8 * i + 4 * h) * 2), cb = *(LAS u32x2*)(size_t)(L_MOD + 2048 + (n0 + 8 * i + 4 * h) * 2);
#pragma unroll
      for (int q = 0; q < 4; q++) {
        float gq = q & 1 ? bfhi(ga[q >> 1]) : bflo(ga[q >> 1]), cq = q & 1 ? bfhi(cb[q >> 1]) : bflo(cb[q >> 1]);
        float xq = q & 1 ? bfhi(xres[i][q >> 1]) : bflo(xres[i][q >> 1]);
        float v = rbf(fmaf(gq, a0[4 * i + q] + o[q], xq));
        x1[4 * i + q] = v; s1 += v; s2 += v * v; xs[4 * i + q] = v * cq;
      }
    }
    __builtin_amdgcn_raw_buffer_store_b128(pk8(xs, 0), rs, l * 16, WS_XS + (tile2 * 32 + (n0 >> 4)) * 1024, SC1);
    __builtin_amdgcn_raw_buffer_store_b128(pk8(xs, 8), rs, l * 16, WS_XS + (tile2 * 32 + (n0 >> 4) + 1) * 1024, SC1);
    s1 = sum32(s1); s2 = sum32(s2);
    if (l < 32) __builtin_amdgcn_raw_buffer_store_b64(u32x2{__float_as_uint(s1), __float_as_uint(s2)}, rs, l * 8, WS_ST2 + (tile2 * 16 + (n0 >> 5)) * 256, SC1);
    dma_tile<2752>(wg, du_of(m, 1) * 32, R0, w, l);
    dma_tile<2752>(wu, du_of(m, 1) * 32, R1, w, l);
    VMCNT(16);
    set_flag(rs, FL_X + tile2 * 16 + (n0 >> 5), tag, l);
    if (b == 0 && t == 0) __builtin_amdgcn_raw_buffer_store_b32(tag, rs, WS_EPOCH, 0, SC1);
  }
  TS(14);
  const bool three = m < 11;
  if (w < 3 && (three || w < 2)) {
    u32x4 a;
    FENCE();
    for (;;) {
      a = __builtin_amdgcn_raw_buffer_load_b128(rs, WS_UVG + (h * 1376 + du_of(m, w) * 32 + r) * 16, 0, SC1);
      if (__all(a[1] == tag && a[3] == tag) || budget == 0) break;
      budget--;
      FENCE();
    }
    *(LAS unsigned*)(size_t)(L_UV + t * 4) = a[0];
    *(LAS unsigned*)(size_t)(L_UV + 768 + t * 4) = a[2];
  }
  wait_flags(rs, FL_X + tile * 16, 16, tag, l, budget);
  TS(15);
  float mean2, rstd2;
  {
    u32x2 st[8];
#pragma unroll
    for (int i = 0; i < 8; i++) st[i] = __builtin_amdgcn_raw_buffer_load_b64(rs, (h * 8 + i) * 256 + r * 8, WS_ST2 + tile * 16 * 256, SC1);
#pragma unroll
    for (int s = 0; s < 32; s++) af[s] = __builtin_amdgcn_raw_buffer_load_b128(rs, l * 16, WS_XS + (tile * 32 + s) * 1024, SC1);
    float s1 = 0, s2 = 0;
#pragma unroll
    for (int i = 0; i < 8; i++) { s1 += __uint_as_float(st[i][0]); s2 += __uint_as_float(st[i][1]); }
    mean2 = sum32(s1) * (1.f / 512);
    rstd2 = __builtin_amdgcn_rsqf(fmaxf(sum32(s2) * (1.f / 512) - mean2 * mean2, 0.f) + 1e-5f);
  }
  const float nm = -mean2 * rstd2;
  const unsigned uvb = opq(L_UV + 16 * h);
  f32x4 ug, uu, vg, vu; float eg, eu; unsigned hv[8]; float hlo;
  auto ep = [&](int k, int p, const f32x16& ag, const f32x16& au) {
    const int e = k >> 2, i = e >> 2, q = e & 3;
    if ((k & 15) == 0) {
      const unsigned o = uvb + (p * 64 + 8 * i) * 4;
      ug = *(LAS f32x4*)(size_t)o; uu = *(LAS f32x4*)(size_t)(o + 128); vg = *(LAS f32x4*)(size_t)(o + 768); vu = *(LAS f32x4*)(size_t)(o + 896);
    }
    if ((k & 3) == 1) eg = -1.44269504088896f * fmaf(ag[e], rstd2, fmaf(nm, ug[q], vg[q]));
    if ((k & 3) == 2) { eg = 1.f + __builtin_amdgcn_exp2f(eg); eu = fmaf(au[e], rstd2, fmaf(nm, uu[q], vu[q])); }
    if ((k & 3) == 3) {
      float v = eu * __builtin_amdgcn_rcpf(eg);
      if (e & 1) hv[e >> 1] = pk(hlo, v); else hlo = v;
      if ((e & 7) == 7) __builtin_amdgcn_raw_buffer_store_b128(u32x4{hv[(e >> 1) - 3], hv[(e >> 1) - 2], hv[(e >> 1) - 1], hv[e >> 1]}, rs, l * 16,
                                                               WS_HID + (tile * 86 + du_of(m, p) * 2 + (e >> 3)) * 1024, SC1);
    }
  };
  {
    f32x16 g0 = Z, u0 = Z, g1 = Z, u1 = Z;
    gemm2<32, 1024, 512, 0>(R3 + trp, R2 + trp, af, g0, u0, NoFill());
    TS(16);
    BAR();
    if (three) { dma_tile<2752>(wg, du_of(m, 2) * 32, R3, w, l); dma_tile<2752>(wu, du_of(m, 2) * 32, R2, w, l); }
    dma_wd<0>(wd, ct, 2, R4, w, l);
    gemm2<32, 1024, 512, 0>(R0 + trp, R1 + trp, af, g1, u1, [&](int k) { ep(k, 0, g0, u0); });
    VMCNT(0);
    set_flag(rs, FL_D + tile * 48 + m, tag, l);
    TS(17);
    BAR();
    dma_wd16(wd, ct, 0, R0, w, l);
    if (three) {
      g0 = Z; u0 = Z;
      gemm2<32, 1024, 512, 0>(R3 + trp, R2 + trp, af, g0, u0, [&](int k) { ep(k, 1, g1, u1); });
      g1 = g0; u1 = u0;
      VMCNT(0);
      set_flag(rs, FL_D + tile * 48 + 16 + m, tag, l);
    }
    VMCNT(0);
    TS(18);
    BAR();
    const int pl = three ? 2 : 1;
#pragma unroll
    for (int k = 0; k < 64; k++) ep(k, pl, g1, u1);
    dma_wd16(wd, ct, 1, R2, w, l);
    VMCNT(16);
    set_flag(rs, FL_D + tile * 48 + pl * 16 + m, tag, l);
  }
  TS(19);
  {
    f32x16 a0 = Z, a1 = Z;
    const unsigned b0 = trp + kh * 1024 + kh * 2048, b1 = trp + kh * 1024 + (1 - kh) * 2048;
    const unsigned hb = WS_HID + (tile2 * 86 + kh) * 1024;
    wait_flags(rs, FL_D + tile2 * 48, 32, tag, l, budget);
    TS(20);
#pragma unroll
    for (int s = 0; s < 32; s++) af[s] = __builtin_amdgcn_raw_buffer_load_b128(rs, l * 16, hb + du_of(s & 15, s >> 4) * 2048, SC1);
    gemm2<16, 4096, 512, 0>(R0 + b1, R0 + b0, af, a1, a0, NoFill());
    TS(21);
    BAR();
    dma_wd<1>(wd, ct, 2, R0, w, l);
    wait_flags(rs, FL_D + tile2 * 48 + 32, 11, tag, l, budget);
    TS(22);
#pragma unroll
    for (int s = 0; s < 11; s++) af[s] = __builtin_amdgcn_raw_buffer_load_b128(rs, l * 16, hb + du_of(s, 2) * 2048, SC1);
    gemm2<16, 4096, 512, 0>(R2 + b1, R2 + b0, af + 16, a1, a0, NoFill());
    BAR();
    gemm2<6, 4096, 512, 0>(R4 + b1, R4 + b0, af, a1, a0, NoFill());
    gemm2<5, 4096, 512, 0>(R0 + b1, R0 + b0, af + 6, a1, a0, NoFill());
#pragma unroll
    for (int i = 0; i < 4; i++) *(LAS f32x4*)(size_t)(R2 + (w ^ 2) * 4096 + i * 1024 + l * 16) = f32x4{a1[4 * i], a1[4 * i + 1], a1[4 * i + 2], a1[4 * i + 3]};
    TS(23);
    BAR();
#pragma unroll
    for (int i = 0; i < 4; i++) {
      f32x4 o = *(LAS f32x4*)(size_t)(R2 + w * 4096 + i * 1024 + l * 16);
      u32x2 gb = *(LAS u32x2*)(size_t)(L_MOD + 3072 + (n0 + 8 * i + 4 * h) * 2);
      u32x2 y = {pk(fmaf(bflo(gb[0]), a0[4 * i] + o[0], x1[4 * i]), fmaf(bfhi(gb[0]), a0[4 * i + 1] + o[1], x1[4 * i + 1])),
                 pk(fmaf(bflo(gb[1]), a0[4 * i + 2] + o[2], x1[4 * i + 2]), fmaf(bfhi(gb[1]), a0[4 * i + 3] + o[3], x1[4 * i + 3]))};
      *(u32x2*)(out + (size_t)(tile2 * 32 + r) * 512 + n0 + 8 * i + 4 * h) = y;
    }
  }
  TS(24);
}

extern "C" void kernel_launch(void* const* d_in, const int* in_sizes, int n_in, void* d_out, int out_size, void* d_ws, size_t ws_size,
                              hipStream_t stream) {
  (void)hipFuncSetAttribute((const void*)fused_block, hipFuncAttributeMaxDynamicSharedMemorySize, 163840);
  fused_block<<<256, 256, 163840, stream>>>((const u16*)d_in[0], (const u16*)d_in[1], (const u16*)d_in[2], (const u16*)d_in[3], (const u16*)d_in[5],
                                            (const u16*)d_in[6], (const u16*)d_in[7], (const u16*)d_in[8], (const u16*)d_in[9], (u16*)d_out, d_ws);
}
```

```cpp
#include <hip/hip_runtime.h>
#include <hip/hip_bf16.h>

typedef __bf16 bf16x8 __attribute__((ext_vector_type(8)));
typedef __bf16 bf16x2 __attribute__((ext_vector_type(2)));
typedef short s16x4 __attribute__((ext_vector_type(4)));
typedef float f32x16 __attribute__((ext_vector_type(16)));
typedef float f32x4 __attribute__((ext_vector_type(4)));
typedef float f32x2 __attribute__((ext_vector_type(2)));
typedef unsigned u32x4 __attribute__((ext_vector_type(4)));
typedef unsigned u32x2 __attribute__((ext_vector_type(2)));
typedef unsigned short u16;
typedef _Float16 f16x2 __attribute__((ext_vector_type(2)));
#define LAS __attribute__((address_space(3)))
#define DEV __device__ __forceinline__

constexpr unsigned WS_EPOCH = 0;
constexpr unsigned WS_MODG = 4096;
constexpr unsigned WS_UVG  = WS_MODG + 40960;
constexpr unsigned WS_FLG  = WS_UVG + 45056;
constexpr unsigned FL_H = 0;
constexpr unsigned FL_O = FL_H + 32 * 32;
constexpr unsigned FL_X = FL_O + 64 * 16;
constexpr unsigned FL_D = FL_X + 64 * 16;
constexpr unsigned FL_N = FL_D + 64 * 48;
constexpr unsigned WS_KVP  = WS_FLG + FL_N * 64;
constexpr unsigned WS_ST2  = WS_KVP + 256 * 4352;
constexpr unsigned WS_H1   = WS_ST2 + 64 * 16 * 256;
constexpr unsigned WS_O    = WS_H1 + (2u << 20);
constexpr unsigned WS_XS   = WS_O + (2u << 20);
constexpr unsigned WS_HID  = WS_XS + (2u << 20);
constexpr unsigned WS_END  = WS_HID + 64 * 86 * 1024;
constexpr unsigned WS_DBG  = 32u << 20;
constexpr unsigned R0 = 0, R1 = 32768, R2 = 65536, R3 = 98304, R4 = 131072;
constexpr unsigned L_MOD = 155648;
constexpr unsigned L_UV = L_MOD + 4096;
constexpr unsigned L_RED = L_UV + 1536;
constexpr unsigned L_END = L_RED + 2048;
constexpr unsigned L_KVS = R4 + 18432, L_ZS = L_KVS + 2048, L_PHI = L_ZS + 1024;
static_assert(L_END <= 163840, "lds");

DEV float bflo(unsigned u) { return __uint_as_float(u << 16); }
DEV float bfhi(unsigned u) { return __uint_as_float(u & 0xffff0000u); }
DEV unsigned pk(float a, float b) { bf16x2 v = {(__bf16)a, (__bf16)b}; return __builtin_bit_cast(unsigned, v); }
DEV float rbf(float a) { return (float)(__bf16)a; }
DEV f32x16 mfma(u32x4 a, u32x4 b, f32x16 c) {
  return __builtin_amdgcn_mfma_f32_32x32x16_bf16(__builtin_bit_cast(bf16x8, a), __builtin_bit_cast(bf16x8, b), c, 0, 0, 0);
}
DEV u32x4 pk8(const f32x16& a, int o) {
  return u32x4{pk(a[o], a[o + 1]), pk(a[o + 2], a[o + 3]), pk(a[o + 4], a[o + 5]), pk(a[o + 6], a[o + 7])};
}
template <int ESTEP> DEV u32x4 wfrag(unsigned addr) {
  s16x4 a = __builtin_amdgcn_ds_read_tr16_b64_v4i16((LAS s16x4*)(size_t)addr);
  s16x4 b = __builtin_amdgcn_ds_read_tr16_b64_v4i16((LAS s16x4*)(size_t)(addr + ESTEP));
  u32x2 x = __builtin_bit_cast(u32x2, a), y = __builtin_bit_cast(u32x2, b);
  return u32x4{x[0], x[1], y[0], y[1]};
}
DEV unsigned opq(unsigned v) { asm volatile("" : "+v"(v)); return v; }
struct NoFill { DEV void operator()(int) const {} };
template <int N, int STEP, int ESTEP, int MODE, class F>
DEV void gemm2(unsigned ba, unsigned bb, u32x4* af, f32x16& ca, f32x16& cb, F fill) {
  ba = opq(ba); bb = opq(bb);
  constexpr int D = MODE == 2 ? 4 : 2;
  u32x4 fa[D + 1], fb[D + 1];
#pragma unroll
  for (int s = 0; s < D; s++) { fa[s] = wfrag<ESTEP>(ba + s * STEP); if (MODE != 2) fb[s] = wfrag<ESTEP>(bb + s * STEP); }
#pragma unroll
  for (int s = 0; s < N; s++) {
    __builtin_amdgcn_sched_barrier(0);
    ca = mfma(fa[s % (D + 1)], af[s], ca);
    asm volatile("" : "+v"(ca));
    if (s + D < N) fa[(s + D) % (D + 1)] = wfrag<ESTEP>(ba + (s + D) * STEP);
    fill(2 * s);
    if (MODE != 2) {
      __builtin_amdgcn_sched_barrier(0);
      cb = MODE == 1 ? mfma(af[s], fb[s % (D + 1)], cb) : mfma(fb[s % (D + 1)], af[s], cb);
      asm volatile("" : "+v"(cb));
      if (s + D < N) fb[(s + D) % (D + 1)] = wfrag<ESTEP>(bb + (s + D) * STEP);
      fill(2 * s + 1);
    }
  }
  __builtin_amdgcn_sched_barrier(0);
}
template <int CTRL> DEV float dpp(float v) {
  return __int_as_float(__builtin_amdgcn_update_dpp(0, __float_as_int(v), CTRL, 0xf, 0xf, true));
}
DEV float sum32(float v) { u32x2 r = __builtin_amdgcn_permlane32_swap(__float_as_uint(v), __float_as_uint(v), false, false); return __uint_as_float(r[0]) + __uint_as_float(r[1]); }
DEV float sum16(float v) { u32x2 r = __builtin_amdgcn_permlane16_swap(__float_as_uint(v), __float_as_uint(v), false, false); return __uint_as_float(r[0]) + __uint_as_float(r[1]); }
DEV float max32(float v) { u32x2 r = __builtin_amdgcn_permlane32_swap(__float_as_uint(v), __float_as_uint(v), false, false); return fmaxf(__uint_as_float(r[0]), __uint_as_float(r[1])); }
DEV float wavesum(float v) { v += dpp<0xB1>(v); v += dpp<0x4E>(v); v += dpp<0x124>(v); v += dpp<0x128>(v); return sum32(sum16(v)); }
#define DMAI "global_load_lds_dwordx4 %1, %2\n\t"
template <int VSTEP, int LSTEP, int PACE> DEV void dman(const void* sbase, unsigned voff, unsigned lds, int n) {
  unsigned keep; lds = __builtin_amdgcn_readfirstlane(lds);
  asm volatile("s_mov_b32 %0, m0\n\ts_mov_b32 m0, %3\n\ts_nop 2\n"
               "1:\n\ts_nop %c7\n\ts_waitcnt vmcnt(60)\n\t" DMAI "v_add_u32 %1, %5, %1\n\ts_add_u32 m0, m0, %6\n\ts_sub_u32 %4, %4, 1\n\ts_cbranch_scc0 1b\n\ts_mov_b32 m0, %0"
               : "=&s"(keep), "+v"(voff), "+s"(sbase), "+s"(lds), "+s"(n) : "i"(VSTEP), "i"(LSTEP), "i"(PACE) : "memory", "scc");
}
template <int PITCH, int PACE> DEV void dma_tile(const u16* src, int col, unsigned lds, int l) {
  dman<16 * PITCH, 1024, PACE>(src, (l >> 2) * PITCH + col * 2 + (l & 3) * 16, lds, 31);
}
DEV int du_of(int m, int p) { return m < 11 ? 3 * m + p : 11 + 2 * m + p; }
template <int PACE> DEV void dma_wd(const u16* src, int ct, int p, int m0, int m1, unsigned lds, int l) {
#pragma unroll
  for (int q = 0; q < 4; q++) {
    const unsigned voff = (du_of(m0, p) * 32 + q * 8 + ((l >> 2) & 7)) * 1024 + ct * 128 + (l >> 5) * 64 + (l & 3) * 16;
    if (m0 < 11) dman<3 * 32768, 4096, PACE>(src, voff, lds + q * 1024, m1 - m0 - 1);
    else dman<2 * 32768, 4096, PACE>(src, voff, lds + q * 1024, m1 - m0 - 1);
  }
}
#define VMCNT(n) asm volatile("s_waitcnt vmcnt(" #n ")" ::: "memory")
#define BAR() asm volatile("s_waitcnt lgkmcnt(0)\n\ts_barrier" ::: "memory")
#define FENCE() asm volatile("" ::: "memory")
constexpr int SC1 = 16;
#define PA 8
#define PB 2
#define KF 18
#define PREN 12
constexpr int PRE = PREN;
#define LDF(base) __builtin_amdgcn_raw_buffer_load_b128(rs, l * 16, (base), SC1)
constexpr unsigned SPIN_MAX = 1u << 18;
#define TS(i)

DEV void wait_flags(__amdgpu_buffer_rsrc_t rs, unsigned first, int n, unsigned tag, int l, unsigned& budget) {
  unsigned off = WS_FLG + (first + (l < n ? l : 0)) * 64;
  FENCE();
  for (;;) {
    unsigned v = __builtin_amdgcn_raw_buffer_load_b32(rs, off, 0, SC1);
    if (__all(v == tag) || budget == 0) break;
    budget--;
    FENCE();
  }
  FENCE();
}
DEV void set_flag(__amdgpu_buffer_rsrc_t rs, unsigned idx, unsigned tag, int l) {
  if (l == 0) __builtin_amdgcn_raw_buffer_store_b32(tag, rs, WS_FLG + idx * 64, 0, SC1);
  FENCE();
}
DEV void gemv_red(float (&a)[8], unsigned col0, int t) {
#pragma unroll
  for (int j = 0; j < 8; j++) { a[j] += dpp<0x4E>(a[j]); a[j] += dpp<0x124>(a[j]); a[j] += dpp<0x128>(a[j]); }
  if ((t & 15) < 2) {
    LAS f32x4* p = (LAS f32x4*)(size_t)(L_RED + ((t >> 4) * 32 + col0 + (t & 1) * 8) * 4);
    p[0] = f32x4{a[0], a[1], a[2], a[3]}; p[1] = f32x4{a[4], a[5], a[6], a[7]};
  }
}
DEV void gemv_acc(float (&a)[8], u32x4 v, float s) {
#pragma unroll
  for (int j = 0; j < 4; j++) { a[2 * j] += s * bflo(v[j]); a[2 * j + 1] += s * bfhi(v[j]); }
}
DEV float red_col(int col, int half) {
  float s = 0;
#pragma unroll
  for (int r = 0; r < 8; r++) s += *(LAS float*)(size_t)(L_RED + ((half * 8 + r) * 32 + col) * 4);
  return sum32(s);
}
DEV void phi(const f32x16& x, int o, u32x4& pp, u32x4& pm) {
  float mx = x[o], mn = x[o];
#pragma unroll
  for (int j = 1; j < 8; j++) { mx = fmaxf(mx, x[o + j]); mn = fminf(mn, x[o + j]); }
  mx = max32(mx); mn = -max32(-mn);
  const float L = 1.44269504088896f;
  float ep[8], em[8], sp = 0, sm = 0, a = -mx * L, c = mn * L;
#pragma unroll
  for (int j = 0; j < 8; j++) {
    ep[j] = __builtin_amdgcn_exp2f(fmaf(x[o + j], L, a)); em[j] = __builtin_amdgcn_exp2f(fmaf(x[o + j], -L, c));
    sp += ep[j]; sm += em[j];
  }
  sp = __builtin_amdgcn_rcpf(sum32(sp)); sm = __builtin_amdgcn_rcpf(sum32(sm));
#pragma unroll
  for (int j = 0; j < 4; j++) { pp[j] = pk(ep[2 * j] * sp, ep[2 * j + 1] * sp); pm[j] = pk(em[2 * j] * sm, em[2 * j + 1] * sm); }
}

__global__ __launch_bounds__(320) void fused_block(const u16* __restrict__ x, const u16* __restrict__ wqkv, const u16* __restrict__ wphi,
    const u16* __restrict__ wo, const u16* __restrict__ wg, const u16* __restrict__ wu, const u16* __restrict__ wd,
    const u16* __restrict__ cvec, const u16* __restrict__ wmod, u16* __restrict__ out, void* ws) {
  extern __shared__ __attribute__((aligned(16))) char smem[];
  const int t = threadIdx.x, l = t & 63, w = __builtin_amdgcn_readfirstlane(t >> 6), b = blockIdx.x;
  const int xc = b & 7, j = b >> 3, g = xc * 2 + (j >> 4), m = j & 15, h = l >> 5, r = l & 31;
  const int th = m >> 3, ct = m & 7, tt = w & 1, kh = w >> 1;
  const __amdgpu_buffer_rsrc_t rs = __builtin_amdgcn_make_buffer_rsrc(ws, 0, WS_END, 0x00020000);
  unsigned budget = SPIN_MAX;
  const f32x16 Z = {0, 0, 0, 0, 0, 0, 0, 0, 0, 0, 0, 0, 0, 0, 0, 0};
  TS(0); TS(1);
  const int m1 = (xc & 3) * 4 + (j & 3), g1 = (xc >> 2) * 8 + (j >> 2), m3 = (xc & 1) * 8 + (j & 7), g3 = (xc >> 1) * 4 + (j >> 3);
  const bool three = m3 < 11;
  if (w == 4) {
    const unsigned ltag = __builtin_amdgcn_raw_buffer_load_b32(rs, WS_EPOCH, 0, SC1) + 1;
    const int iu = xc * 22 + j;
    const bool do_uv = j < 22 && iu < 172;
    u32x4 wuv[16];
    __builtin_amdgcn_s_sleep(4);
    __builtin_amdgcn_s_setprio(3);
    dman<0, 0, 0>(wphi, m1 * 1024 + l * 16, L_PHI, 0);
    dma_tile<3072, PA>(wqkv, 512 + m1 * 32, R0, l);
    BAR(); BAR();
    dma_tile<3072, PA>(wqkv, 1024 + m1 * 32, R1, l);
    VMCNT(0); BAR();
    dma_tile<3072, PA>(wqkv, m1 * 32, R2, l);
    asm volatile("" :: "v"(ltag));
    if (do_uv) {
      const __amdgpu_buffer_rsrc_t rw = __builtin_amdgcn_make_buffer_rsrc((void*)(iu < 86 ? wg : wu), 0, 512 * 2752, 0x00020000);
#pragma unroll
      for (int i = 0; i < 16; i++) { wuv[i] = __builtin_amdgcn_raw_buffer_load_b128(rw, (l >> 1) * 16 * 2752 + (l & 1) * 16, (iu < 86 ? iu : iu - 86) * 32 + i * 2752, 0); asm volatile("s_sleep 2" ::: "memory"); }
    }
    dma_tile<2752, PA>(wg, du_of(m3, 0) * 32, R3, l);
    VMCNT(32);
    BAR();
    dma_tile<1024, PB>(wo, ct * 64, R0, l); dma_tile<1024, PB>(wo, ct * 64 + 32, R1, l);
    BAR();
    dma_tile<2752, PB>(wu, du_of(m3, 0) * 32, R2, l);
    VMCNT(32); BAR();
    __builtin_amdgcn_s_setprio(0);
    if (do_uv) {
      float a[8] = {0, 0, 0, 0, 0, 0, 0, 0}, c[8] = {0, 0, 0, 0, 0, 0, 0, 0};
#pragma unroll
      for (int i = 0; i < 2; i++) {
        const u32x4 c1 = *(LAS u32x4*)(size_t)(L_MOD + 2048 + (l >> 1) * 32 + i * 16), sb = *(LAS u32x4*)(size_t)(L_MOD + 1024 + (l >> 1) * 32 + i * 16);
#pragma unroll
        for (int q = 0; q < 8; q++) {
          gemv_acc(a, wuv[8 * i + q], q & 1 ? bfhi(c1[q >> 1]) : bflo(c1[q >> 1]));
          gemv_acc(c, wuv[8 * i + q], q & 1 ? bfhi(sb[q >> 1]) : bflo(sb[q >> 1]));
        }
      }
#pragma unroll
      for (int q = 0; q < 8; q++) {
        a[q] += dpp<0x4E>(a[q]); a[q] += dpp<0x124>(a[q]); a[q] += dpp<0x128>(a[q]); a[q] = sum32(sum16(a[q]));
        c[q] += dpp<0x4E>(c[q]); c[q] += dpp<0x124>(c[q]); c[q] += dpp<0x128>(c[q]); c[q] = sum32(sum16(c[q]));
      }
      float ua = a[0], va = c[0];
#pragma unroll
      for (int q = 1; q < 8; q++) { ua = (l >> 1) == q ? a[q] : ua; va = (l >> 1) == q ? c[q] : va; }
      VMCNT(0);
      if (l < 16) __builtin_amdgcn_raw_buffer_store_b128(u32x4{__float_as_uint(ua), ltag, __float_as_uint(va), ltag}, rs, WS_UVG + (iu * 16 + (l & 1) * 8 + (l >> 1)) * 16, 0, SC1);
    }
    else VMCNT(0);
    __builtin_amdgcn_s_setprio(3);
    BAR();
    dma_tile<2752, PA>(wg, du_of(m3, 1) * 32, R0, l); dma_tile<2752, PA>(wu, du_of(m3, 1) * 32, R1, l);
    VMCNT(0); BAR();
    if (three) { dma_tile<2752, 0>(wg, du_of(m3, 2) * 32, R3, l); dma_tile<2752, 0>(wu, du_of(m3, 2) * 32, R2, l); }
    VMCNT(0); BAR();
    dma_wd<0>(wd, ct, 0, 0, 11, R0, l); dma_wd<0>(wd, ct, 0, 11, 16, R0 + 11 * 4096, l);
    dma_wd<0>(wd, ct, 2, 0, 6, R4, l);
    VMCNT(24); BAR();
    dma_wd<0>(wd, ct, 1, 0, 11, R2, l); dma_wd<0>(wd, ct, 1, 11, 16, R2 + 11 * 4096, l);
    VMCNT(0); BAR();
    dma_wd<0>(wd, ct, 2, 6, 11, R0, l);
    VMCNT(0); BAR();
    BAR();
    return;
  }
  const int grow = (t >> 1) * 4, gcol = (t & 1) * 8, im = xc * 24 + j;
  const int srow = g * 128 + m * 8 + w * 2;
  u32x4 wa[4]; u32x2 cv;
  const u32x4 xr0 = *(const u32x4*)(x + (size_t)srow * 512 + l * 8), xr1 = *(const u32x4*)(x + (size_t)(srow + 1) * 512 + l * 8);
  const unsigned tagv = __builtin_amdgcn_raw_buffer_load_b32(rs, WS_EPOCH, 0, SC1);
  if (j < 24) {
    cv = *(const u32x2*)(cvec + grow);
#pragma unroll
    for (int i = 0; i < 4; i++) wa[i] = *(const u32x4*)(wmod + (size_t)(grow + i) * 3072 + im * 16 + gcol);
  }
  FENCE();
  {
    float v0[8], v1[8], s0 = 0, s1 = 0;
#pragma unroll
    for (int i = 0; i < 4; i++) { v0[2 * i] = bflo(xr0[i]); v0[2 * i + 1] = bfhi(xr0[i]); v1[2 * i] = bflo(xr1[i]); v1[2 * i + 1] = bfhi(xr1[i]); }
#pragma unroll
    for (int i = 0; i < 8; i++) { s0 += v0[i]; s1 += v1[i]; }
    s0 = wavesum(s0) * (1.f / 512); s1 = wavesum(s1) * (1.f / 512);
    float q0 = 0, q1 = 0;
#pragma unroll
    for (int i = 0; i < 8; i++) { v0[i] -= s0; v1[i] -= s1; q0 += v0[i] * v0[i]; q1 += v1[i] * v1[i]; }
    q0 = __builtin_amdgcn_rsqf(wavesum(q0) * (1.f / 512) + 1e-5f); q1 = __builtin_amdgcn_rsqf(wavesum(q1) * (1.f / 512) + 1e-5f);
    u32x4 y0, y1;
#pragma unroll
    for (int i = 0; i < 4; i++) {
      y0[i] = __builtin_bit_cast(unsigned, f16x2{(_Float16)(v0[2 * i] * q0), (_Float16)(v0[2 * i + 1] * q0)});
      y1[i] = __builtin_bit_cast(unsigned, f16x2{(_Float16)(v1[2 * i] * q1), (_Float16)(v1[2 * i + 1] * q1)});
    }
    *(LAS u32x4*)(size_t)(R4 + (w * 2) * 1040 + l * 16) = y0;
    *(LAS u32x4*)(size_t)(R4 + (w * 2 + 1) * 1040 + l * 16) = y1;
  }
  const unsigned tag = __builtin_amdgcn_readfirstlane(tagv) + 1;
  BAR();
  TS(2);
#pragma unroll
  for (int i = 0; i < 2; i++) {
    const int c = w * 16 + i * 8 + (l >> 3);
    __builtin_amdgcn_raw_buffer_store_b128(*(LAS u32x4*)(size_t)(R4 + (l & 7) * 1040 + c * 16), rs, c * 512 + ((m & 3) * 8 + (l & 7)) * 16,
                                           WS_H1 + (g * 4 + (m >> 2)) * 32768, SC1);
  }
  FENCE();
  if (j < 24) {
    float a[8] = {0, 0, 0, 0, 0, 0, 0, 0};
#pragma unroll
    for (int i = 0; i < 4; i++) {
      float c = i & 1 ? bfhi(cv[i >> 1]) : bflo(cv[i >> 1]);
      gemv_acc(a, wa[i], rbf(c * __builtin_amdgcn_rcpf(1.f + __builtin_amdgcn_exp2f(-1.44269504088896f * c))));
    }
    gemv_red(a, 0, t);
  }
  BAR();
  TS(3);
  VMCNT(0);
  if (j < 24 && w == 0) {
    float v = red_col(r & 15, h), v1 = dpp<0xB1>(v);
    if (l < 16 && !(l & 1)) __builtin_amdgcn_raw_buffer_store_b64(u32x2{pk(v, v1), tag}, rs, WS_MODG + (im * 8 + (l >> 1)) * 8, 0, SC1);
  }
  set_flag(rs, FL_H + (g * 4 + (m >> 2)) * 16 + (m & 3) * 4 + w, tag, l);
  TS(4);
  const int tile = g1 * 4 + w, tile3 = g3 * 4 + w;
  const int tile2 = g * 4 + th * 2 + tt;
  const int n0 = ct * 64 + kh * 32;
  const unsigned trb = ((l & 15) >> 2) * 64 + ((l >> 4) & 1) * 32 + (l & 3) * 8;
  const unsigned trn = trb + h * 512, trp = trb + h * 256;
  u32x4 af[32];
  {
    u32x4 a; unsigned f;
    const unsigned fo = WS_FLG + (FL_H + tile * 16 + (l & 15)) * 64;
    FENCE();
    for (;;) {
      a = __builtin_amdgcn_raw_buffer_load_b128(rs, WS_MODG + t * 16, 0, SC1);
      f = __builtin_amdgcn_raw_buffer_load_b32(rs, fo, 0, SC1);
      if (__all(a[1] == tag && a[3] == tag && f == tag) || budget == 0) break;
      budget--;
      FENCE();
    }
    FENCE();
    TS(5);
#pragma unroll
    for (int s = 0; s < PRE; s++) af[s] = LDF(WS_H1 + (tile * 32 + s) * 1024);
    f32x4 v = {bflo(a[0]), bfhi(a[0]), bflo(a[2]), bfhi(a[2])};
    if (w >= 2) v = f32x4{rbf(1.f + v[0]), rbf(1.f + v[1]), rbf(1.f + v[2]), rbf(1.f + v[3])};
    *(LAS f32x4*)(size_t)(L_MOD + t * 16) = v;
  }
  BAR();
  TS(6);
  u32x4 ph0, ph1, idp, idm;
  {
    f32x16 ak = Z, av = Z;
    const unsigned mb = opq(L_MOD + h * 32);
    f32x4 ms[3][2];
    auto ldm = [&](int u) {
      ms[u % 3][0] = *(LAS f32x4*)(size_t)(mb + (u >> 1) * 64 + (u & 1) * 16); ms[u % 3][1] = *(LAS f32x4*)(size_t)(mb + (u >> 1) * 64 + (u & 1) * 16 + 2048);
    };
    auto mod = [&](int u) {
      const unsigned ya = af[u >> 1][2 * (u & 1)], yb = af[u >> 1][2 * (u & 1) + 1]; unsigned oa, ob; float t0, t1, t2, t3;
      const f32x4 c = ms[u % 3][1], d = ms[u % 3][0];
      asm volatile("v_fma_mix_f32 %2, %6, %8, %12 op_sel_hi:[1,0,0]\n\tv_fma_mix_f32 %3, %6, %9, %13 op_sel:[1,0,0] op_sel_hi:[1,0,0]\n\t"
                   "v_fma_mix_f32 %4, %7, %10, %14 op_sel_hi:[1,0,0]\n\tv_fma_mix_f32 %5, %7, %11, %15 op_sel:[1,0,0] op_sel_hi:[1,0,0]\n\t"
                   "v_cvt_pk_bf16_f32 %0, %2, %3\n\tv_cvt_pk_bf16_f32 %1, %4, %5"
                   : "=v"(oa), "=v"(ob), "=&v"(t0), "=&v"(t1), "=&v"(t2), "=&v"(t3)
                   : "v"(ya), "v"(yb), "v"(c[0]), "v"(c[1]), "v"(c[2]), "v"(c[3]), "v"(d[0]), "v"(d[1]), "v"(d[2]), "v"(d[3]));
      af[u >> 1][2 * (u & 1)] = oa; af[u >> 1][2 * (u & 1) + 1] = ob;
    };
    ldm(0); ldm(1); ldm(2); mod(0); ldm(3); mod(1); ldm(4); mod(2);
    gemm2<32, 1024, 256, 1>(R0 + trn, R1 + trn, af, ak, av, [&](int k) {
      if (!(k & 1) && (k >> 1) + PRE < 32) af[(k >> 1) + PRE] = LDF(WS_H1 + (tile * 32 + (k >> 1) + PRE) * 1024);
      if (k + 5 < 64) ldm(k + 5);
      if (k + 3 < 64) mod(k + 3);
    });
    {
      const u32x4 v = wfrag<256>(L_PHI + ((l >> 4) & 1) * 512 + (4 * h + ((l & 15) >> 2)) * 32 + (l & 3) * 8);
#pragma unroll
      for (int i = 0; i < 4; i++) {
        ph0[i] = r < 16 ? v[i] : 0; ph1[i] = r < 16 ? 0 : v[i];
        int k0 = 8 * (i >> 1) + 4 * h + 2 * (i & 1);
        unsigned id = ((r & 15) == k0 ? 0x3f80u : 0) | ((r & 15) == k0 + 1 ? 0x3f800000u : 0);
        idp[i] = r < 16 ? id : 0; idm[i] = r < 16 ? 0 : id;
      }
    }
    TS(7);
    f32x16 pkk = mfma(ph1, pk8(ak, 8), mfma(ph0, pk8(ak, 0), Z));
    u32x4 v0 = pk8(av, 0), v1 = pk8(av, 8);
    const u32x4 ones = {0x3f803f80u, 0x3f803f80u, 0x3f803f80u, 0x3f803f80u};
    u32x4 pp, pm;
    phi(pkk, 0, pp, pm);
    f32x16 tr = mfma(pm, idm, mfma(pp, idp, Z));
    f32x16 kv0 = mfma(pk8(tr, 8), r < 16 ? v1 : ones, mfma(pk8(tr, 0), r < 16 ? v0 : ones, Z));
    phi(pkk, 8, pp, pm);
    tr = mfma(pm, idm, mfma(pp, idp, Z));
    f32x16 kv1 = mfma(pk8(tr, 8), r < 16 ? ones : v1, mfma(pk8(tr, 0), r < 16 ? ones : v0, Z));
#pragma unroll
    for (int i = 0; i < 4; i++) {
      f32x4 a, z;
#pragma unroll
      for (int q = 0; q < 4; q++) { a[q] = r < 16 ? kv0[4 * i + q] : kv1[4 * i + q]; z[q] = r < 16 ? kv1[4 * i + q] : kv0[4 * i + q]; }
      *(LAS f32x4*)(size_t)(R4 + w * 4352 + i * 1024 + l * 16) = a;
      if ((l & 15) == 0) *(LAS f32x4*)(size_t)(R4 + w * 4352 + 4096 + ((l >> 4) * 4 + i) * 16) = z;
    }
  }
  BAR();
  {
    f32x4 a = {0, 0, 0, 0}, z = {0, 0, 0, 0};
#pragma unroll
    for (int i = 0; i < 4; i++) { a += *(LAS f32x4*)(size_t)(R4 + i * 4352 + t * 16); z += *(LAS f32x4*)(size_t)(R4 + i * 4352 + 4096 + (t & 15) * 16); }
    const unsigned base = WS_KVP + (m1 * 16 + g1) * 4352;
    __builtin_amdgcn_raw_buffer_store_b128(u32x4{pk(a[0], a[1]), tag, pk(a[2], a[3]), tag}, rs, base + t * 16, 0, SC1);
    if (t < 16) __builtin_amdgcn_raw_buffer_store_b128(u32x4{pk(z[0], z[1]), tag, pk(z[2], z[3]), tag}, rs, base + 4096 + t * 16, 0, SC1);
  }
  TS(8);
  u32x4 ma = __builtin_amdgcn_raw_buffer_load_b128(rs, WS_MODG + 4096 + t * 16, 0, SC1), mc = __builtin_amdgcn_raw_buffer_load_b128(rs, WS_MODG + 8192 + t * 16, 0, SC1);
  u32x4 p[16], z;
  auto ldkv = [&](int i) { p[i] = __builtin_amdgcn_raw_buffer_load_b128(rs, t * 16, WS_KVP + (m1 * 16 + i) * 4352, SC1); };
  auto ldz = [&]() { z = __builtin_amdgcn_raw_buffer_load_b128(rs, (l & 15) * 4352 + 4096 + ((l >> 4) * 4 + w) * 16, WS_KVP + m1 * 16 * 4352, SC1); };
  FENCE();
  u32x4 qp0, qm0, qp1, qm1;
  {
    f32x16 aq = Z;
    gemm2<32, 1024, 256, 2>(R2 + trn, 0, af, aq, aq, NoFill());
    f32x16 pq = mfma(ph1, pk8(aq, 8), mfma(ph0, pk8(aq, 0), Z));
#pragma unroll
    for (int i = 0; i < 8; i++) ldkv(i);
    FENCE();
    phi(pq, 0, qp0, qm0);
    asm volatile("" : "+v"(qp0), "+v"(qm0) :: "memory");
#pragma unroll
    for (int i = 8; i < 16; i++) ldkv(i);
    ldz();
    FENCE();
    phi(pq, 8, qp1, qm1);
    asm volatile("" : "+v"(qp1), "+v"(qm1) :: "memory");
  }
  TS(9);
  {
    u32x4 a = ma, c = mc;
    while (!__all(a[1] == tag && a[3] == tag && c[1] == tag && c[3] == tag) && budget != 0) {
      budget--;
      FENCE();
      a = __builtin_amdgcn_raw_buffer_load_b128(rs, WS_MODG + 4096 + t * 16, 0, SC1);
      c = __builtin_amdgcn_raw_buffer_load_b128(rs, WS_MODG + 8192 + t * 16, 0, SC1);
    }
    if (t < 128) { c[0] = pk(1.f + bflo(c[0]), 1.f + bfhi(c[0])); c[2] = pk(1.f + bflo(c[2]), 1.f + bfhi(c[2])); }
    *(LAS u32x2*)(size_t)(L_MOD + t * 8) = u32x2{a[0], a[2]};
    *(LAS u32x2*)(size_t)(L_MOD + 2048 + t * 8) = u32x2{c[0], c[2]};
  }
  {
    f32x4 a = {0, 0, 0, 0};
    unsigned todo = 0xf;
    for (;;) {
#pragma unroll
      for (int q = 0; q < 4; q++)
        if (todo >> q & 1) {
          bool ok = true;
#pragma unroll
          for (int i = 4 * q; i < 4 * q + 4; i++) ok = ok && p[i][1] == tag && p[i][3] == tag;
          if (__all(ok)) {
            todo &= ~(1u << q);
            f32x4 c = {0, 0, 0, 0};
#pragma unroll
            for (int i = 4 * q; i < 4 * q + 4; i++) c += f32x4{bflo(p[i][0]), bfhi(p[i][0]), bflo(p[i][2]), bfhi(p[i][2])};
            p[4 * q] = __builtin_bit_cast(u32x4, c);
          }
        }
      if ((todo == 0 && __all(z[1] == tag && z[3] == tag)) || budget == 0) break;
      budget--;
      FENCE();
      ldz();
#pragma unroll
      for (int q = 0; q < 4; q++)
        if (todo >> q & 1) { ldkv(4 * q); ldkv(4 * q + 1); ldkv(4 * q + 2); ldkv(4 * q + 3); }
    }
    TS(10);
#pragma unroll
    for (int q = 0; q < 4; q++) { const u32x4 c = p[4 * q]; a += __builtin_bit_cast(f32x4, c); }
    *(LAS u32x2*)(size_t)(L_KVS + l * 32 + w * 8) = u32x2{pk(a[0], a[1]), pk(a[2], a[3])};
    f32x4 zz = {bflo(z[0]), bfhi(z[0]), bflo(z[2]), bfhi(z[2])};
#pragma unroll
    for (int i = 0; i < 4; i++) { zz[i] += dpp<0xB1>(zz[i]); zz[i] += dpp<0x4E>(zz[i]); zz[i] += dpp<0x124>(zz[i]); zz[i] += dpp<0x128>(zz[i]); }
    if ((l & 15) == 0) *(LAS u32x2*)(size_t)(L_ZS + (l >> 4) * 32 + w * 8) = u32x2{pk(zz[0], zz[1]), pk(zz[2], zz[3])};
  }
  u32x4 xres[2];
#pragma unroll
  for (int i = 0; i < 2; i++) xres[i] = __builtin_amdgcn_raw_buffer_load_b128(__builtin_amdgcn_make_buffer_rsrc((void*)x, 0, 2u << 20, 0x00020000),
      (l >> 2) * 1024 + (l & 3) * 16, (tile2 * 32 + i * 16) * 1024 + n0 * 2, 0);
  BAR();
  {
    u32x4 k0 = *(LAS u32x4*)(size_t)(L_KVS + l * 32), k1 = *(LAS u32x4*)(size_t)(L_KVS + l * 32 + 16);
    u32x4 z0 = *(LAS u32x4*)(size_t)(L_ZS + (l >> 4) * 32), z1 = *(LAS u32x4*)(size_t)(L_ZS + (l >> 4) * 32 + 16);
    f32x16 o0 = mfma(r < 16 ? k1 : z1, qm0, mfma(r < 16 ? k0 : z0, qp0, Z));
    f32x16 o1 = mfma(r < 16 ? z1 : k1, qm1, mfma(r < 16 ? z0 : k0, qp1, Z));
    float d0 = __builtin_amdgcn_rcpf(o0[8] + 1e-6f), d1 = __builtin_amdgcn_rcpf(o1[0] + 1e-6f);
    o0 *= d0; o1 *= d1;
    __builtin_amdgcn_raw_buffer_store_b128(pk8(o0, 0), rs, l * 16, WS_O + (tile * 32 + 2 * m1) * 1024, SC1);
    __builtin_amdgcn_raw_buffer_store_b128(pk8(o1, 8), rs, l * 16, WS_O + (tile * 32 + 2 * m1 + 1) * 1024, SC1);
  }
  VMCNT(0);
  set_flag(rs, FL_O + tile * 16 + m1, tag, l);
  TS(11);
  float xv[16], gav[16], cbv[16];
#pragma unroll
  for (int i = 0; i < 2; i++) *(LAS u32x4*)(size_t)(R4 + w * 4096 + (i * 16 + (l >> 2)) * 80 + (l & 3) * 16) = xres[i];
#pragma unroll
  for (int i = 0; i < 4; i++) {
    u32x2 xx = *(LAS u32x2*)(size_t)(R4 + w * 4096 + r * 80 + i * 16 + h * 8);
    u32x2 ga = *(LAS u32x2*)(size_t)(L_MOD + (n0 + 8 * i + 4 * h) * 2), cb = *(LAS u32x2*)(size_t)(L_MOD + 2048 + (n0 + 8 * i + 4 * h) * 2);
#pragma unroll
    for (int q = 0; q < 4; q++) {
      xv[4 * i + q] = q & 1 ? bfhi(xx[q >> 1]) : bflo(xx[q >> 1]); gav[4 * i + q] = q & 1 ? bfhi(ga[q >> 1]) : bflo(ga[q >> 1]);
      cbv[4 * i + q] = q & 1 ? bfhi(cb[q >> 1]) : bflo(cb[q >> 1]);
      asm volatile("" : "+v"(xv[4 * i + q]), "+v"(gav[4 * i + q]), "+v"(cbv[4 * i + q]));
    }
  }
  BAR();
  unsigned x1[8];
  wait_flags(rs, FL_O + tile2 * 16 + kh * 8, 8, tag, l, budget);
  TS(12);
  {
#pragma unroll
    for (int s = 0; s < PRE; s++) af[s] = LDF(WS_O + (tile2 * 32 + kh * 16 + s) * 1024);
    f32x16 a0 = Z, a1 = Z;
    const unsigned b0 = trp + kh * (16384 + 32768), b1 = trp + kh * 16384 + (1 - kh) * 32768;
    gemm2<16, 1024, 512, 0>(b1, b0, af, a1, a0, [&](int k) { if (k + PRE < 16) af[k + PRE] = LDF(WS_O + (tile2 * 32 + kh * 16 + k + PRE) * 1024); });
#pragma unroll
    for (int i = 0; i < 4; i++) *(LAS f32x4*)(size_t)(R4 + (w ^ 2) * 4096 + i * 1024 + l * 16) = f32x4{a1[4 * i], a1[4 * i + 1], a1[4 * i + 2], a1[4 * i + 3]};
    TS(13);
    BAR();
    float s1 = 0, s2 = 0; f32x16 xs;
#pragma unroll
    for (int i = 0; i < 4; i++) {
      f32x4 o = *(LAS f32x4*)(size_t)(R4 + w * 4096 + i * 1024 + l * 16);
#pragma unroll
      for (int q = 0; q < 2; q++) {
        const int e = 4 * i + 2 * q;
        const unsigned v = pk(fmaf(gav[e], a0[e] + o[2 * q], xv[e]), fmaf(gav[e + 1], a0[e + 1] + o[2 * q + 1], xv[e + 1]));
        const float v0 = bflo(v), v1 = bfhi(v);
        x1[e >> 1] = v; s1 += v0 + v1; s2 = fmaf(v0, v0, fmaf(v1, v1, s2)); xs[e] = v0 * cbv[e]; xs[e + 1] = v1 * cbv[e + 1];
      }
    }
    __builtin_amdgcn_raw_buffer_store_b128(pk8(xs, 0), rs, l * 16, WS_XS + (tile2 * 32 + (n0 >> 4)) * 1024, SC1);
    __builtin_amdgcn_raw_buffer_store_b128(pk8(xs, 8), rs, l * 16, WS_XS + (tile2 * 32 + (n0 >> 4) + 1) * 1024, SC1);
    s1 = sum32(s1); s2 = sum32(s2);
    if (l < 32) __builtin_amdgcn_raw_buffer_store_b64(u32x2{__float_as_uint(s1), __float_as_uint(s2)}, rs, l * 8, WS_ST2 + (tile2 * 16 + (n0 >> 5)) * 256, SC1);
    VMCNT(0);
    set_flag(rs, FL_X + tile2 * 16 + (n0 >> 5), tag, l);
    if (b == 0 && t == 0) __builtin_amdgcn_raw_buffer_store_b32(tag, rs, WS_EPOCH, 0, SC1);
  }
  TS(14);
  if (w < 3 && (three || w < 2)) {
    u32x4 a;
    FENCE();
    for (;;) {
      a = __builtin_amdgcn_raw_buffer_load_b128(rs, WS_UVG + (h * 1376 + du_of(m3, w) * 32 + r) * 16, 0, SC1);
      if (__all(a[1] == tag && a[3] == tag) || budget == 0) break;
      budget--;
      FENCE();
    }
    const float sc = h ? 1.f : -1.44269504088896f;
    *(LAS f32x2*)(size_t)(L_UV + (w * 32 + r) * 16 + h * 8) = f32x2{__uint_as_float(a[0]) * sc, __uint_as_float(a[2]) * sc};
  }
  wait_flags(rs, FL_X + tile3 * 16, 16, tag, l, budget);
  TS(15);
  float mean2, rstd2;
  f32x16 ga0 = Z, u0 = Z, ga1 = Z, u1 = Z;
  {
    u32x2 st[8];
#pragma unroll
    for (int s = 0; s < PRE; s++) af[s] = LDF(WS_XS + (tile3 * 32 + s) * 1024);
    FENCE();
    gemm2<32, 1024, 512, 0>(R3 + trp, R2 + trp, af, ga0, u0, [&](int k) {
      if (!(k & 1) && (k >> 1) + PRE < 32) af[(k >> 1) + PRE] = LDF(WS_XS + (tile3 * 32 + (k >> 1) + PRE) * 1024);
      if (k >= 40 && k < 48) st[k - 40] = __builtin_amdgcn_raw_buffer_load_b64(rs, (h * 8 + k - 40) * 256 + r * 8, WS_ST2 + tile3 * 16 * 256, SC1);
    });
    float s1 = 0, s2 = 0;
#pragma unroll
    for (int i = 0; i < 8; i++) { s1 += __uint_as_float(st[i][0]); s2 += __uint_as_float(st[i][1]); }
    mean2 = sum32(s1) * (1.f / 512);
    rstd2 = __builtin_amdgcn_rsqf(fmaxf(sum32(s2) * (1.f / 512) - mean2 * mean2, 0.f) + 1e-5f);
  }
  const float nm = -mean2 * rstd2, rl = -1.44269504088896f * rstd2;
  const unsigned uvb = opq(L_UV + 64 * h);
  f32x4 uv[2]; float eg, eu; unsigned hv[8]; float hlo;
  auto ep = [&](int k, int p, const f32x16& ag, const f32x16& au, bool pin) {
    if (k >= 51) return;
    if (k % 3 == 0 && k < 48) uv[(k / 3) & 1] = *(LAS f32x4*)(size_t)(uvb + (p * 32 + 8 * (k / 12) + (k / 3 & 3)) * 16);
    if (k < 3) return;
    const int e = k / 3 - 1, c = k % 3;
    const f32x4 d = uv[e & 1];
    if (c == 0) { eg = __builtin_amdgcn_exp2f(fmaf(ag[e], rl, fmaf(nm, d[0], d[1]))); if (pin) asm volatile("" : "+v"(eg)); }
    if (c == 1) { eg = __builtin_amdgcn_rcpf(1.f + eg); eu = fmaf(nm, d[2], d[3]); if (pin) asm volatile("" : "+v"(eg), "+v"(eu)); }
    if (c == 2) {
      float v = fmaf(au[e], rstd2, eu) * eg;
      if (e & 1) hv[e >> 1] = pin ? opq(pk(hlo, v)) : pk(hlo, v); else { hlo = v; if (pin) asm volatile("" : "+v"(hlo)); }
      if ((e & 7) == 7) __builtin_amdgcn_raw_buffer_store_b128(u32x4{hv[(e >> 1) - 3], hv[(e >> 1) - 2], hv[(e >> 1) - 1], hv[e >> 1]}, rs, l * 16,
                                                               WS_HID + (tile3 * 86 + du_of(m3, p) * 2 + (e >> 3)) * 1024, SC1);
    }
  };
  {
    TS(16);
    BAR();
    gemm2<32, 1024, 512, 0>(R0 + trp, R1 + trp, af, ga1, u1, [&](int k) { ep(k, 0, ga0, u0, true); });
    TS(17);
    BAR();
    const unsigned hb = WS_HID + (tile2 * 86 + kh) * 1024;
    const unsigned fd = WS_FLG + (FL_D + tile2 * 48 + (l & 15)) * 64;
    unsigned fv = 0;
    if (three) {
      ga0 = Z; u0 = Z;
      gemm2<32, 1024, 512, 0>(R3 + trp, R2 + trp, af, ga0, u0, [&](int k) {
        if (k == 10) { VMCNT(0); set_flag(rs, FL_D + tile3 * 48 + m3, tag, l); }
        ep(k, 1, ga1, u1, true);
        if (k == 56) { fv = __builtin_amdgcn_raw_buffer_load_b32(rs, fd, 0, SC1); FENCE(); }
      });
      ga1 = ga0; u1 = u0;
    }
    TS(18);
    BAR();
    const int pl = three ? 2 : 1;
#pragma unroll
    for (int k = 0; k < 24; k++) ep(k, pl, ga1, u1, false);
    asm volatile("" : "+v"(hv[0]), "+v"(hv[1]), "+v"(hv[2]));
    VMCNT(0);
    set_flag(rs, FL_D + tile3 * 48 + (pl - 1) * 16 + m3, tag, l);
    const bool early = three && __all(fv == tag);
    if (early) {
#pragma unroll
      for (int s = 0; s < 8; s++) af[s] = LDF(hb + du_of(s, 0) * 2048);
    }
    FENCE();
#pragma unroll
    for (int k = 24; k < 64; k++) ep(k, pl, ga1, u1, false);
    FENCE();
    if (!early) {
      if (!three) { VMCNT(0); set_flag(rs, FL_D + tile3 * 48 + 16 + m3, tag, l); }
      wait_flags(rs, FL_D + tile2 * 48, 16, tag, l, budget);
#pragma unroll
      for (int s = 0; s < 8; s++) af[s] = LDF(hb + du_of(s, 0) * 2048);
    }
    TS(19);
    f32x16 a0 = Z, a1 = Z;
    const unsigned b0 = trp + kh * 2048 + kh * 512, b1 = trp + kh * 2048 + (1 - kh) * 512;
    gemm2<16, 4096, 1024, 0>(R0 + b1, R0 + b0, af, a1, a0, [&](int k) {
      if (k < 8) af[8 + k] = LDF(hb + du_of(8 + k, 0) * 2048);
      if (k == KF) {
        VMCNT(0);
        if (three) set_flag(rs, FL_D + tile3 * 48 + 32 + m3, tag, l);
        fv = __builtin_amdgcn_raw_buffer_load_b32(rs, fd + 16 * 64, 0, SC1); FENCE();
      }
    });
    TS(20);
    if (!__all(fv == tag)) wait_flags(rs, FL_D + tile2 * 48 + 16, 16, tag, l, budget);
    FENCE();
#pragma unroll
    for (int s = 0; s < 12; s++) af[16 + s] = LDF(hb + du_of(s, 1) * 2048);
    BAR();
    TS(21);
    gemm2<16, 4096, 1024, 0>(R2 + b1, R2 + b0, af + 16, a1, a0, [&](int k) {
      if (k < 4) af[28 + k] = LDF(hb + du_of(12 + k, 1) * 2048);
      if (k == 4) { fv = __builtin_amdgcn_raw_buffer_load_b32(rs, WS_FLG + (FL_D + tile2 * 48 + 32 + (l < 11 ? l : 0)) * 64, 0, SC1); FENCE(); }
      if (k == 20) { if (!__all(fv == tag)) wait_flags(rs, FL_D + tile2 * 48 + 32, 11, tag, l, budget); FENCE(); }
      if (k >= 20 && k < 31) af[k - 20] = LDF(hb + du_of(k - 20, 2) * 2048);
    });
    TS(22);
    BAR();
    gemm2<6, 4096, 1024, 0>(R4 + b1, R4 + b0, af, a1, a0, NoFill());
    gemm2<5, 4096, 1024, 0>(R0 + b1, R0 + b0, af + 6, a1, a0, NoFill());
    const int l2 = opq(l);
#pragma unroll
    for (int i = 0; i < 4; i++) *(LAS f32x4*)(size_t)(R2 + (w ^ 2) * 4096 + i * 1024 + l2 * 16) = f32x4{a1[4 * i], a1[4 * i + 1], a1[4 * i + 2], a1[4 * i + 3]};
    TS(23);
    BAR();
#pragma unroll
    for (int i = 0; i < 4; i++) {
      f32x4 o = *(LAS f32x4*)(size_t)(R2 + w * 4096 + i * 1024 + l2 * 16);
      u32x2 gb = *(LAS u32x2*)(size_t)(L_MOD + 3072 + (n0 + 8 * i + 4 * (l2 >> 5)) * 2);
      u32x2 y = {pk(fmaf(bflo(gb[0]), a0[4 * i] + o[0], bflo(x1[2 * i])), fmaf(bfhi(gb[0]), a0[4 * i + 1] + o[1], bfhi(x1[2 * i]))),
                 pk(fmaf(bflo(gb[1]), a0[4 * i + 2] + o[2], bflo(x1[2 * i + 1])), fmaf(bfhi(gb[1]), a0[4 * i + 3] + o[3], bfhi(x1[2 * i + 1])))};
      *(LAS u32x2*)(size_t)(R3 + w * 4096 + (l2 & 31) * 80 + i * 16 + (l2 >> 5) * 8) = y;
    }
#pragma unroll
    for (int i = 0; i < 2; i++)
      *(u32x4*)(out + (size_t)(tile2 * 32 + i * 16 + (l2 >> 2)) * 512 + n0 + (l2 & 3) * 8) = *(LAS u32x4*)(size_t)(R3 + w * 4096 + (i * 16 + (l2 >> 2)) * 80 + (l2 & 3) * 16);
  }
  TS(24); TS(25);
}

extern "C" void kernel_launch(void* const* d_in, const int* in_sizes, int n_in, void* d_out, int out_size, void* d_ws, size_t ws_size,
                              hipStream_t stream) {
  (void)hipFuncSetAttribute((const void*)fused_block, hipFuncAttributeMaxDynamicSharedMemorySize, 163840);
  fused_block<<<256, 320, 163840, stream>>>((const u16*)d_in[0], (const u16*)d_in[1], (const u16*)d_in[2], (const u16*)d_in[3], (const u16*)d_in[5],
                                            (const u16*)d_in[6], (const u16*)d_in[7], (const u16*)d_in[8], (const u16*)d_in[9], (u16*)d_out, d_ws);
}
```

```cpp
#include <hip/hip_runtime.h>
#include <hip/hip_bf16.h>

typedef __bf16 bf16x8 __attribute__((ext_vector_type(8)));
typedef __bf16 bf16x2 __attribute__((ext_vector_type(2)));
typedef short s16x4 __attribute__((ext_vector_type(4)));
typedef float f32x16 __attribute__((ext_vector_type(16)));
typedef float f32x4 __attribute__((ext_vector_type(4)));
typedef float f32x2 __attribute__((ext_vector_type(2)));
typedef unsigned u32x4 __attribute__((ext_vector_type(4)));
typedef unsigned u32x2 __attribute__((ext_vector_type(2)));
typedef unsigned short u16;
typedef _Float16 f16x2 __attribute__((ext_vector_type(2)));
#define LAS __attribute__((address_space(3)))
#define DEV __device__ __forceinline__

constexpr unsigned WS_EPOCH = 0;
constexpr unsigned WS_MODG = 4096;
constexpr unsigned WS_UVG  = WS_MODG + 40960;
constexpr unsigned WS_FLG  = WS_UVG + 45056;
constexpr unsigned FL_H = 0;
constexpr unsigned FL_O = FL_H + 32 * 32;
constexpr unsigned FL_X = FL_O + 64 * 16;
constexpr unsigned FL_D = FL_X + 64 * 16;
constexpr unsigned FL_N = FL_D + 64 * 48;
constexpr unsigned WS_KVP  = WS_FLG + FL_N * 64;
constexpr unsigned WS_ST2  = WS_KVP + 256 * 4352;
constexpr unsigned WS_H1   = WS_ST2 + 64 * 16 * 256;
constexpr unsigned WS_O    = WS_H1 + (2u << 20);
constexpr unsigned WS_XS   = WS_O + (2u << 20);
constexpr unsigned WS_HID  = WS_XS + (2u << 20);
constexpr unsigned WS_END  = WS_HID + 64 * 86 * 1024;
constexpr unsigned WS_DBG  = 32u << 20;
constexpr unsigned R0 = 0, R1 = 32768, R2 = 65536, R3 = 98304, R4 = 131072;
constexpr unsigned L_MOD = 155648;
constexpr unsigned L_UV = L_MOD + 4096;
constexpr unsigned L_RED = L_UV + 1536;
constexpr unsigned L_END = L_RED + 2048;
constexpr unsigned L_KVS = R4 + 18432, L_ZS = L_KVS + 2048, L_PHI = L_ZS + 1024;
static_assert(L_END <= 163840, "lds");

DEV float bflo(unsigned u) { return __uint_as_float(u << 16); }
DEV float bfhi(unsigned u) { return __uint_as_float(u & 0xffff0000u); }
DEV unsigned pk(float a, float b) { bf16x2 v = {(__bf16)a, (__bf16)b}; return __builtin_bit_cast(unsigned, v); }
DEV float rbf(float a) { return (float)(__bf16)a; }
DEV f32x16 mfma(u32x4 a, u32x4 b, f32x16 c) {
  return __builtin_amdgcn_mfma_f32_32x32x16_bf16(__builtin_bit_cast(bf16x8, a), __builtin_bit_cast(bf16x8, b), c, 0, 0, 0);
}
DEV u32x4 pk8(const f32x16& a, int o) {
  return u32x4{pk(a[o], a[o + 1]), pk(a[o + 2], a[o + 3]), pk(a[o + 4], a[o + 5]), pk(a[o + 6], a[o + 7])};
}
template <int ESTEP> DEV u32x4 wfrag(unsigned addr) {
  s16x4 a = __builtin_amdgcn_ds_read_tr16_b64_v4i16((LAS s16x4*)(size_t)addr);
  s16x4 b = __builtin_amdgcn_ds_read_tr16_b64_v4i16((LAS s16x4*)(size_t)(addr + ESTEP));
  u32x2 x = __builtin_bit_cast(u32x2, a), y = __builtin_bit_cast(u32x2, b);
  return u32x4{x[0], x[1], y[0], y[1]};
}
DEV unsigned opq(unsigned v) { asm volatile("" : "+v"(v)); return v; }
struct NoFill { DEV void operator()(int) const {} };
template <int N, int STEP, int ESTEP, int MODE, class F>
DEV void gemm2(unsigned ba, unsigned bb, u32x4* af, f32x16& ca, f32x16& cb, F fill) {
  ba = opq(ba); bb = opq(bb);
  constexpr int D = MODE == 2 ? 4 : 2;
  u32x4 fa[D + 1], fb[D + 1];
#pragma unroll
  for (int s = 0; s < D; s++) { fa[s] = wfrag<ESTEP>(ba + s * STEP); if (MODE != 2) fb[s] = wfrag<ESTEP>(bb + s * STEP); }
#pragma unroll
  for (int s = 0; s < N; s++) {
    __builtin_amdgcn_sched_barrier(0);
    ca = mfma(fa[s % (D + 1)], af[s], ca);
    asm volatile("" : "+v"(ca));
    if (s + D < N) fa[(s + D) % (D + 1)] = wfrag<ESTEP>(ba + (s + D) * STEP);
    fill(2 * s);
    if (MODE != 2) {
      __builtin_amdgcn_sched_barrier(0);
      cb = MODE == 1 ? mfma(af[s], fb[s % (D + 1)], cb) : mfma(fb[s % (D + 1)], af[s], cb);
      asm volatile("" : "+v"(cb));
      if (s + D < N) fb[(s + D) % (D + 1)] = wfrag<ESTEP>(bb + (s + D) * STEP);
      fill(2 * s + 1);
    }
  }
  __builtin_amdgcn_sched_barrier(0);
}
template <int CTRL> DEV float dpp(float v) {
  return __int_as_float(__builtin_amdgcn_update_dpp(0, __float_as_int(v), CTRL, 0xf, 0xf, true));
}
DEV float sum32(float v) { u32x2 r = __builtin_amdgcn_permlane32_swap(__float_as_uint(v), __float_as_uint(v), false, false); return __uint_as_float(r[0]) + __uint_as_float(r[1]); }
DEV float sum16(float v) { u32x2 r = __builtin_amdgcn_permlane16_swap(__float_as_uint(v), __float_as_uint(v), false, false); return __uint_as_float(r[0]) + __uint_as_float(r[1]); }
DEV float max32(float v) { u32x2 r = __builtin_amdgcn_permlane32_swap(__float_as_uint(v), __float_as_uint(v), false, false); return fmaxf(__uint_as_float(r[0]), __uint_as_float(r[1])); }
DEV float wavesum(float v) { v += dpp<0xB1>(v); v += dpp<0x4E>(v); v += dpp<0x124>(v); v += dpp<0x128>(v); return sum32(sum16(v)); }
#define DMAI "global_load_lds_dwordx4 %1, %2\n\t"
template <int VSTEP, int LSTEP, int PACE> DEV void dman(const void* sbase, unsigned voff, unsigned lds, int n) {
  unsigned keep; lds = __builtin_amdgcn_readfirstlane(lds);
  asm volatile("s_mov_b32 %0, m0\n\ts_mov_b32 m0, %3\n\ts_nop 2\n"
               "1:\n\ts_nop %c7\n\ts_waitcnt vmcnt(60)\n\t" DMAI "v_add_u32 %1, %5, %1\n\ts_add_u32 m0, m0, %6\n\ts_sub_u32 %4, %4, 1\n\ts_cbranch_scc0 1b\n\ts_mov_b32 m0, %0"
               : "=&s"(keep), "+v"(voff), "+s"(sbase), "+s"(lds), "+s"(n) : "i"(VSTEP), "i"(LSTEP), "i"(PACE) : "memory", "scc");
}
template <int PITCH, int PACE> DEV void dma_tile(const u16* src, int col, unsigned lds, int l) {
  dman<16 * PITCH, 1024, PACE>(src, (l >> 2) * PITCH + col * 2 + (l & 3) * 16, lds, 31);
}
DEV int du_of(int m, int p) { return m < 11 ? 3 * m + p : 11 + 2 * m + p; }
template <int PACE> DEV void dma_wd(const u16* src, int ct, int p, int m0, int m1, unsigned lds, int l) {
#pragma unroll
  for (int q = 0; q < 4; q++) {
    const unsigned voff = (du_of(m0, p) * 32 + q * 8 + ((l >> 2) & 7)) * 1024 + ct * 128 + (l >> 5) * 64 + (l & 3) * 16;
    if (m0 < 11) dman<3 * 32768, 4096, PACE>(src, voff, lds + q * 1024, m1 - m0 - 1);
    else dman<2 * 32768, 4096, PACE>(src, voff, lds + q * 1024, m1 - m0 - 1);
  }
}
#define VMCNT(n) asm volatile("s_waitcnt vmcnt(" #n ")" ::: "memory")
#define BAR() asm volatile("s_waitcnt lgkmcnt(0)\n\ts_barrier" ::: "memory")
#define FENCE() asm volatile("" ::: "memory")
constexpr int SC1 = 16;
#define PA 8
#define PB 2
#define KF 18
#define PREN 12
constexpr int PRE = PREN;
#define LDF(base) __builtin_amdgcn_raw_buffer_load_b128(rs, l * 16, (base), SC1)
constexpr unsigned SPIN_MAX = 1u << 18;
#define TS(i)

DEV void wait_flags(__amdgpu_buffer_rsrc_t rs, unsigned first, int n, unsigned tag, int l, unsigned& budget) {
  unsigned off = WS_FLG + (first + (l < n ? l : 0)) * 64;
  FENCE();
  for (;;) {
    unsigned v = __builtin_amdgcn_raw_buffer_load_b32(rs, off, 0, SC1);
    if (__all(v == tag) || budget == 0) break;
    budget--;
    FENCE();
  }
  FENCE();
}
DEV void set_flag(__amdgpu_buffer_rsrc_t rs, unsigned idx, unsigned tag, int l) {
  if (l == 0) __builtin_amdgcn_raw_buffer_store_b32(tag, rs, WS_FLG + idx * 64, 0, SC1);
  FENCE();
}
DEV void gemv_red(float (&a)[8], unsigned col0, int t) {
#pragma unroll
  for (int j = 0; j < 8; j++) { a[j] += dpp<0x4E>(a[j]); a[j] += dpp<0x124>(a[j]); a[j] += dpp<0x128>(a[j]); }
  if ((t & 15) < 2) {
    LAS f32x4* p = (LAS f32x4*)(size_t)(L_RED + ((t >> 4) * 32 + col0 + (t & 1) * 8) * 4);
    p[0] = f32x4{a[0], a[1], a[2], a[3]}; p[1] = f32x4{a[4], a[5], a[6], a[7]};
  }
}
DEV void gemv_acc(float (&a)[8], u32x4 v, float s) {
#pragma unroll
  for (int j = 0; j < 4; j++) { a[2 * j] += s * bflo(v[j]); a[2 * j + 1] += s * bfhi(v[j]); }
}
DEV float red_col(int col, int half) {
  float s = 0;
#pragma unroll
  for (int r = 0; r < 8; r++) s += *(LAS float*)(size_t)(L_RED + ((half * 8 + r) * 32 + col) * 4);
  return sum32(s);
}
DEV void phi(const f32x16& x, int o, u32x4& pp, u32x4& pm) {
  float mx = x[o], mn = x[o];
#pragma unroll
  for (int j = 1; j < 8; j++) { mx = fmaxf(mx, x[o + j]); mn = fminf(mn, x[o + j]); }
  mx = max32(mx); mn = -max32(-mn);
  const float L = 1.44269504088896f;
  float ep[8], em[8], sp = 0, sm = 0, a = -mx * L, c = mn * L;
#pragma unroll
  for (int j = 0; j < 8; j++) {
    ep[j] = __builtin_amdgcn_exp2f(fmaf(x[o + j], L, a)); em[j] = __builtin_amdgcn_exp2f(fmaf(x[o + j], -L, c));
    sp += ep[j]; sm += em[j];
  }
  sp = __builtin_amdgcn_rcpf(sum32(sp)); sm = __builtin_amdgcn_rcpf(sum32(sm));
#pragma unroll
  for (int j = 0; j < 4; j++) { pp[j] = pk(ep[2 * j] * sp, ep[2 * j + 1] * sp); pm[j] = pk(em[2 * j] * sm, em[2 * j + 1] * sm); }
}

__global__ __launch_bounds__(320) void fused_block(const u16* __restrict__ x, const u16* __restrict__ wqkv, const u16* __restrict__ wphi,
    const u16* __restrict__ wo, const u16* __restrict__ wg, const u16* __restrict__ wu, const u16* __restrict__ wd,
    const u16* __restrict__ cvec, const u16* __restrict__ wmod, u16* __restrict__ out, void* ws) {
  extern __shared__ __attribute__((aligned(16))) char smem[];
  const int t = threadIdx.x, l = t & 63, w = __builtin_amdgcn_readfirstlane(t >> 6), b = blockIdx.x;
  const int xc = b & 7, j = b >> 3, g = xc * 2 + (j >> 4), m = j & 15, h = l >> 5, r = l & 31;
  const int th = m >> 3, ct = m & 7, tt = w & 1, kh = w >> 1;
  const __amdgpu_buffer_rsrc_t rs = __builtin_amdgcn_make_buffer_rsrc(ws, 0, WS_END, 0x00020000);
  unsigned budget = SPIN_MAX;
  const f32x16 Z = {0, 0, 0, 0, 0, 0, 0, 0, 0, 0, 0, 0, 0, 0, 0, 0};
  TS(0); TS(1);
  const int m1 = (xc & 3) * 4 + (j & 3), g1 = (xc >> 2) * 8 + (j >> 2), m3 = (xc & 1) * 8 + (j & 7), g3 = (xc >> 1) * 4 + (j >> 3);
  const bool three = m3 < 11;
  if (w == 4) {
    const unsigned ltag = __builtin_amdgcn_raw_buffer_load_b32(rs, WS_EPOCH, 0, SC1) + 1;
    const int iu = xc * 22 + j;
    const bool do_uv = j < 22 && iu < 172;
    u32x4 wuv[16];
    unsigned touch;
    {
      const int id = l < 12 ? l >> 1 : 5, idx = j * 2 + (l & 1);
      const size_t base = id == 0 ? (size_t)ws : id == 1 ? (size_t)out : id == 2 ? (size_t)wo : id == 3 ? (size_t)wg : id == 4 ? (size_t)wu : (size_t)wd;
      const unsigned step = id == 0 ? WS_END / 64 & ~63u : id == 1 ? 32768 : id == 2 ? 8192 : 22016;
      touch = *(const __attribute__((address_space(1))) unsigned*)(base + idx * step);
    }
    __builtin_amdgcn_s_sleep(4);
    __builtin_amdgcn_s_setprio(3);
    dman<0, 0, 0>(wphi, m1 * 1024 + l * 16, L_PHI, 0);
    dma_tile<3072, PA>(wqkv, 512 + m1 * 32, R0, l);
    BAR(); BAR();
    dma_tile<3072, PA>(wqkv, 1024 + m1 * 32, R1, l);
    VMCNT(0); BAR();
    dma_tile<3072, PA>(wqkv, m1 * 32, R2, l);
    asm volatile("" :: "v"(ltag), "v"(touch));
    if (do_uv) {
      const __amdgpu_buffer_rsrc_t rw = __builtin_amdgcn_make_buffer_rsrc((void*)(iu < 86 ? wg : wu), 0, 512 * 2752, 0x00020000);
#pragma unroll
      for (int i = 0; i < 16; i++) { wuv[i] = __builtin_amdgcn_raw_buffer_load_b128(rw, (l >> 1) * 16 * 2752 + (l & 1) * 16, (iu < 86 ? iu : iu - 86) * 32 + i * 2752, 0); asm volatile("s_sleep 2" ::: "memory"); }
    }
    dma_tile<2752, PA>(wg, du_of(m3, 0) * 32, R3, l);
    VMCNT(32);
    BAR();
    dma_tile<1024, PB>(wo, ct * 64, R0, l); dma_tile<1024, PB>(wo, ct * 64 + 32, R1, l);
    BAR();
    dma_tile<2752, PB>(wu, du_of(m3, 0) * 32, R2, l);
    VMCNT(32); BAR();
    __builtin_amdgcn_s_setprio(0);
    if (do_uv) {
      float a[8] = {0, 0, 0, 0, 0, 0, 0, 0}, c[8] = {0, 0, 0, 0, 0, 0, 0, 0};
#pragma unroll
      for (int i = 0; i < 2; i++) {
        const u32x4 c1 = *(LAS u32x4*)(size_t)(L_MOD + 2048 + (l >> 1) * 32 + i * 16), sb = *(LAS u32x4*)(size_t)(L_MOD + 1024 + (l >> 1) * 32 + i * 16);
#pragma unroll
        for (int q = 0; q < 8; q++) {
          gemv_acc(a, wuv[8 * i + q], q & 1 ? bfhi(c1[q >> 1]) : bflo(c1[q >> 1]));
          gemv_acc(c, wuv[8 * i + q], q & 1 ? bfhi(sb[q >> 1]) : bflo(sb[q >> 1]));
        }
      }
#pragma unroll
      for (int q = 0; q < 8; q++) {
        a[q] += dpp<0x4E>(a[q]); a[q] += dpp<0x124>(a[q]); a[q] += dpp<0x128>(a[q]); a[q] = sum32(sum16(a[q]));
        c[q] += dpp<0x4E>(c[q]); c[q] += dpp<0x124>(c[q]); c[q] += dpp<0x128>(c[q]); c[q] = sum32(sum16(c[q]));
      }
      float ua = a[0], va = c[0];
#pragma unroll
      for (int q = 1; q < 8; q++) { ua = (l >> 1) == q ? a[q] : ua; va = (l >> 1) == q ? c[q] : va; }
      VMCNT(0);
      if (l < 16) __builtin_amdgcn_raw_buffer_store_b128(u32x4{__float_as_uint(ua), ltag, __float_as_uint(va), ltag}, rs, WS_UVG + (iu * 16 + (l & 1) * 8 + (l >> 1)) * 16, 0, SC1);
    }
    else VMCNT(0);
    __builtin_amdgcn_s_setprio(3);
    BAR();
    dma_tile<2752, PA>(wg, du_of(m3, 1) * 32, R0, l); dma_tile<2752, PA>(wu, du_of(m3, 1) * 32, R1, l);
    VMCNT(0); BAR();
    if (three) { dma_tile<2752, 0>(wg, du_of(m3, 2) * 32, R3, l); dma_tile<2752, 0>(wu, du_of(m3, 2) * 32, R2, l); }
    VMCNT(0); BAR();
    dma_wd<0>(wd, ct, 0, 0, 11, R0, l); dma_wd<0>(wd, ct, 0, 11, 16, R0 + 11 * 4096, l);
    dma_wd<0>(wd, ct, 2, 0, 6, R4, l);
    VMCNT(24); BAR();
    dma_wd<0>(wd, ct, 1, 0, 11, R2, l); dma_wd<0>(wd, ct, 1, 11, 16, R2 + 11 * 4096, l);
    VMCNT(0); BAR();
    dma_wd<0>(wd, ct, 2, 6, 11, R0, l);
    VMCNT(0); BAR();
    BAR();
    return;
  }
  const int grow = (t >> 1) * 4, gcol = (t & 1) * 8, im = xc * 24 + j;
  const int srow = g * 128 + m * 8 + w * 2;
  u32x4 wa[4]; u32x2 cv;
  const u32x4 xr0 = *(const u32x4*)(x + (size_t)srow * 512 + l * 8), xr1 = *(const u32x4*)(x + (size_t)(srow + 1) * 512 + l * 8);
  const unsigned tagv = __builtin_amdgcn_raw_buffer_load_b32(rs, WS_EPOCH, 0, SC1);
  if (j < 24) {
    cv = *(const u32x2*)(cvec + grow);
#pragma unroll
    for (int i = 0; i < 4; i++) wa[i] = *(const u32x4*)(wmod + (size_t)(grow + i) * 3072 + im * 16 + gcol);
  }
  FENCE();
  {
    float v0[8], v1[8], s0 = 0, s1 = 0;
#pragma unroll
    for (int i = 0; i < 4; i++) { v0[2 * i] = bflo(xr0[i]); v0[2 * i + 1] = bfhi(xr0[i]); v1[2 * i] = bflo(xr1[i]); v1[2 * i + 1] = bfhi(xr1[i]); }
#pragma unroll
    for (int i = 0; i < 8; i++) { s0 += v0[i]; s1 += v1[i]; }
    s0 = wavesum(s0) * (1.f / 512); s1 = wavesum(s1) * (1.f / 512);
    float q0 = 0, q1 = 0;
#pragma unroll
    for (int i = 0; i < 8; i++) { v0[i] -= s0; v1[i] -= s1; q0 += v0[i] * v0[i]; q1 += v1[i] * v1[i]; }
    q0 = __builtin_amdgcn_rsqf(wavesum(q0) * (1.f / 512) + 1e-5f); q1 = __builtin_amdgcn_rsqf(wavesum(q1) * (1.f / 512) + 1e-5f);
    u32x4 y0, y1;
#pragma unroll
    for (int i = 0; i < 4; i++) {
      y0[i] = __builtin_bit_cast(unsigned, f16x2{(_Float16)(v0[2 * i] * q0), (_Float16)(v0[2 * i + 1] * q0)});
      y1[i] = __builtin_bit_cast(unsigned, f16x2{(_Float16)(v1[2 * i] * q1), (_Float16)(v1[2 * i + 1] * q1)});
    }
    *(LAS u32x4*)(size_t)(R4 + (w * 2) * 1040 + l * 16) = y0;
    *(LAS u32x4*)(size_t)(R4 + (w * 2 + 1) * 1040 + l * 16) = y1;
  }
  const unsigned tag = __builtin_amdgcn_readfirstlane(tagv) + 1;
  BAR();
  TS(2);
#pragma unroll
  for (int i = 0; i < 2; i++) {
    const int c = w * 16 + i * 8 + (l >> 3);
    __builtin_amdgcn_raw_buffer_store_b128(*(LAS u32x4*)(size_t)(R4 + (l & 7) * 1040 + c * 16), rs, c * 512 + ((m & 3) * 8 + (l & 7)) * 16,
                                           WS_H1 + (g * 4 + (m >> 2)) * 32768, SC1);
  }
  FENCE();
  if (j < 24) {
    float a[8] = {0, 0, 0, 0, 0, 0, 0, 0};
#pragma unroll
    for (int i = 0; i < 4; i++) {
      float c = i & 1 ? bfhi(cv[i >> 1]) : bflo(cv[i >> 1]);
      gemv_acc(a, wa[i], rbf(c * __builtin_amdgcn_rcpf(1.f + __builtin_amdgcn_exp2f(-1.44269504088896f * c))));
    }
    gemv_red(a, 0, t);
  }
  BAR();
  TS(3);
  VMCNT(0);
  if (j < 24 && w == 0) {
    float v = red_col(r & 15, h), v1 = dpp<0xB1>(v);
    if (l < 16 && !(l & 1)) __builtin_amdgcn_raw_buffer_store_b64(u32x2{pk(v, v1), tag}, rs, WS_MODG + (im * 8 + (l >> 1)) * 8, 0, SC1);
  }
  set_flag(rs, FL_H + (g * 4 + (m >> 2)) * 16 + (m & 3) * 4 + w, tag, l);
  TS(4);
  const int tile = g1 * 4 + w, tile3 = g3 * 4 + w;
  const int tile2 = g * 4 + th * 2 + tt;
  const int n0 = ct * 64 + kh * 32;
  const unsigned trb = ((l & 15) >> 2) * 64 + ((l >> 4) & 1) * 32 + (l & 3) * 8;
  const unsigned trn = trb + h * 512, trp = trb + h * 256;
  u32x4 af[32];
  {
    u32x4 a; unsigned f;
    const unsigned fo = WS_FLG + (FL_H + tile * 16 + (l & 15)) * 64;
    FENCE();
    for (;;) {
      a = __builtin_amdgcn_raw_buffer_load_b128(rs, WS_MODG + t * 16, 0, SC1);
      f = __builtin_amdgcn_raw_buffer_load_b32(rs, fo, 0, SC1);
      if (__all(a[1] == tag && a[3] == tag && f == tag) || budget == 0) break;
      budget--;
      FENCE();
    }
    FENCE();
    TS(5);
#pragma unroll
    for (int s = 0; s < PRE; s++) af[s] = LDF(WS_H1 + (tile * 32 + s) * 1024);
    f32x4 v = {bflo(a[0]), bfhi(a[0]), bflo(a[2]), bfhi(a[2])};
    if (w >= 2) v = f32x4{rbf(1.f + v[0]), rbf(1.f + v[1]), rbf(1.f + v[2]), rbf(1.f + v[3])};
    *(LAS f32x4*)(size_t)(L_MOD + t * 16) = v;
  }
  BAR();
  TS(6);
  u32x4 ph0, ph1, idp, idm;
  {
    f32x16 ak = Z, av = Z;
    const unsigned mb = opq(L_MOD + h * 32);
    f32x4 ms[3][2];
    auto ldm = [&](int u) {
      ms[u % 3][0] = *(LAS f32x4*)(size_t)(mb + (u >> 1) * 64 + (u & 1) * 16); ms[u % 3][1] = *(LAS f32x4*)(size_t)(mb + (u >> 1) * 64 + (u & 1) * 16 + 2048);
    };
    auto mod = [&](int u) {
      const unsigned ya = af[u >> 1][2 * (u & 1)], yb = af[u >> 1][2 * (u & 1) + 1]; unsigned oa, ob; float t0, t1, t2, t3;
      const f32x4 c = ms[u % 3][1], d = ms[u % 3][0];
      asm volatile("v_fma_mix_f32 %2, %6, %8, %12 op_sel_hi:[1,0,0]\n\tv_fma_mix_f32 %3, %6, %9, %13 op_sel:[1,0,0] op_sel_hi:[1,0,0]\n\t"
                   "v_fma_mix_f32 %4, %7, %10, %14 op_sel_hi:[1,0,0]\n\tv_fma_mix_f32 %5, %7, %11, %15 op_sel:[1,0,0] op_sel_hi:[1,0,0]\n\t"
                   "v_cvt_pk_bf16_f32 %0, %2, %3\n\tv_cvt_pk_bf16_f32 %1, %4, %5"
                   : "=v"(oa), "=v"(ob), "=&v"(t0), "=&v"(t1), "=&v"(t2), "=&v"(t3)
                   : "v"(ya), "v"(yb), "v"(c[0]), "v"(c[1]), "v"(c[2]), "v"(c[3]), "v"(d[0]), "v"(d[1]), "v"(d[2]), "v"(d[3]));
      af[u >> 1][2 * (u & 1)] = oa; af[u >> 1][2 * (u & 1) + 1] = ob;
    };
    ldm(0); ldm(1); ldm(2); mod(0); ldm(3); mod(1); ldm(4); mod(2);
    gemm2<32, 1024, 256, 1>(R0 + trn, R1 + trn, af, ak, av, [&](int k) {
      if (!(k & 1) && (k >> 1) + PRE < 32) af[(k >> 1) + PRE] = LDF(WS_H1 + (tile * 32 + (k >> 1) + PRE) * 1024);
      if (k + 5 < 64) ldm(k + 5);
      if (k + 3 < 64) mod(k + 3);
    });
    {
      const u32x4 v = wfrag<256>(L_PHI + ((l >> 4) & 1) * 512 + (4 * h + ((l & 15) >> 2)) * 32 + (l & 3) * 8);
#pragma unroll
      for (int i = 0; i < 4; i++) {
        ph0[i] = r < 16 ? v[i] : 0; ph1[i] = r < 16 ? 0 : v[i];
        int k0 = 8 * (i >> 1) + 4 * h + 2 * (i & 1);
        unsigned id = ((r & 15) == k0 ? 0x3f80u : 0) | ((r & 15) == k0 + 1 ? 0x3f800000u : 0);
        idp[i] = r < 16 ? id : 0; idm[i] = r < 16 ? 0 : id;
      }
    }
    TS(7);
    f32x16 pkk = mfma(ph1, pk8(ak, 8), mfma(ph0, pk8(ak, 0), Z));
    u32x4 v0 = pk8(av, 0), v1 = pk8(av, 8);
    const u32x4 ones = {0x3f803f80u, 0x3f803f80u, 0x3f803f80u, 0x3f803f80u};
    u32x4 pp, pm;
    phi(pkk, 0, pp, pm);
    f32x16 tr = mfma(pm, idm, mfma(pp, idp, Z));
    f32x16 kv0 = mfma(pk8(tr, 8), r < 16 ? v1 : ones, mfma(pk8(tr, 0), r < 16 ? v0 : ones, Z));
    phi(pkk, 8, pp, pm);
    tr = mfma(pm, idm, mfma(pp, idp, Z));
    f32x16 kv1 = mfma(pk8(tr, 8), r < 16 ? ones : v1, mfma(pk8(tr, 0), r < 16 ? ones : v0, Z));
#pragma unroll
    for (int i = 0; i < 4; i++) {
      f32x4 a, z;
#pragma unroll
      for (int q = 0; q < 4; q++) { a[q] = r < 16 ? kv0[4 * i + q] : kv1[4 * i + q]; z[q] = r < 16 ? kv1[4 * i + q] : kv0[4 * i + q]; }
      *(LAS f32x4*)(size_t)(R4 + w * 4352 + i * 1024 + l * 16) = a;
      if ((l & 15) == 0) *(LAS f32x4*)(size_t)(R4 + w * 4352 + 4096 + ((l >> 4) * 4 + i) * 16) = z;
    }
  }
  BAR();
  {
    f32x4 a = {0, 0, 0, 0}, z = {0, 0, 0, 0};
#pragma unroll
    for (int i = 0; i < 4; i++) { a += *(LAS f32x4*)(size_t)(R4 + i * 4352 + t * 16); z += *(LAS f32x4*)(size_t)(R4 + i * 4352 + 4096 + (t & 15) * 16); }
    const unsigned base = WS_KVP + (m1 * 16 + g1) * 4352;
    __builtin_amdgcn_raw_buffer_store_b128(u32x4{pk(a[0], a[1]), tag, pk(a[2], a[3]), tag}, rs, base + t * 16, 0, SC1);
    if (t < 16) __builtin_amdgcn_raw_buffer_store_b128(u32x4{pk(z[0], z[1]), tag, pk(z[2], z[3]), tag}, rs, base + 4096 + t * 16, 0, SC1);
  }
  TS(8);
  u32x4 ma = __builtin_amdgcn_raw_buffer_load_b128(rs, WS_MODG + 4096 + t * 16, 0, SC1), mc = __builtin_amdgcn_raw_buffer_load_b128(rs, WS_MODG + 8192 + t * 16, 0, SC1);
  u32x4 p[16], z;
  auto ldkv = [&](int i) { p[i] = __builtin_amdgcn_raw_buffer_load_b128(rs, t * 16, WS_KVP + (m1 * 16 + i) * 4352, SC1); };
  auto ldz = [&]() { z = __builtin_amdgcn_raw_buffer_load_b128(rs, (l & 15) * 4352 + 4096 + ((l >> 4) * 4 + w) * 16, WS_KVP + m1 * 16 * 4352, SC1); };
  FENCE();
  u32x4 qp0, qm0, qp1, qm1;
  {
    f32x16 aq = Z;
    gemm2<32, 1024, 256, 2>(R2 + trn, 0, af, aq, aq, NoFill());
    f32x16 pq = mfma(ph1, pk8(aq, 8), mfma(ph0, pk8(aq, 0), Z));
#pragma unroll
    for (int i = 0; i < 8; i++) ldkv(i);
    FENCE();
    phi(pq, 0, qp0, qm0);
    asm volatile("" : "+v"(qp0), "+v"(qm0) :: "memory");
#pragma unroll
    for (int i = 8; i < 16; i++) ldkv(i);
    ldz();
    FENCE();
    phi(pq, 8, qp1, qm1);
    asm volatile("" : "+v"(qp1), "+v"(qm1) :: "memory");
  }
  TS(9);
  {
    u32x4 a = ma, c = mc;
    while (!__all(a[1] == tag && a[3] == tag && c[1] == tag && c[3] == tag) && budget != 0) {
      budget--;
      FENCE();
      a = __builtin_amdgcn_raw_buffer_load_b128(rs, WS_MODG + 4096 + t * 16, 0, SC1);
      c = __builtin_amdgcn_raw_buffer_load_b128(rs, WS_MODG + 8192 + t * 16, 0, SC1);
    }
    if (t < 128) { c[0] = pk(1.f + bflo(c[0]), 1.f + bfhi(c[0])); c[2] = pk(1.f + bflo(c[2]), 1.f + bfhi(c[2])); }
    *(LAS u32x2*)(size_t)(L_MOD + t * 8) = u32x2{a[0], a[2]};
    *(LAS u32x2*)(size_t)(L_MOD + 2048 + t * 8) = u32x2{c[0], c[2]};
  }
  {
    f32x4 a = {0, 0, 0, 0};
    unsigned todo = 0xf;
    for (;;) {
#pragma unroll
      for (int q = 0; q < 4; q++)
        if (todo >> q & 1) {
          bool ok = true;
#pragma unroll
          for (int i = 4 * q; i < 4 * q + 4; i++) ok = ok && p[i][1] == tag && p[i][3] == tag;
          if (__all(ok)) {
            todo &= ~(1u << q);
            f32x4 c = {0, 0, 0, 0};
#pragma unroll
            for (int i = 4 * q; i < 4 * q + 4; i++) c += f32x4{bflo(p[i][0]), bfhi(p[i][0]), bflo(p[i][2]), bfhi(p[i][2])};
            p[4 * q] = __builtin_bit_cast(u32x4, c);
          }
        }
      if ((todo == 0 && __all(z[1] == tag && z[3] == tag)) || budget == 0) break;
      budget--;
      FENCE();
      ldz();
#pragma unroll
      for (int q = 0; q < 4; q++)
        if (todo >> q & 1) { ldkv(4 * q); ldkv(4 * q + 1); ldkv(4 * q + 2); ldkv(4 * q + 3); }
    }
    TS(10);
#pragma unroll
    for (int q = 0; q < 4; q++) { const u32x4 c = p[4 * q]; a += __builtin_bit_cast(f32x4, c); }
    *(LAS u32x2*)(size_t)(L_KVS + l * 32 + w * 8) = u32x2{pk(a[0], a[1]), pk(a[2], a[3])};
    f32x4 zz = {bflo(z[0]), bfhi(z[0]), bflo(z[2]), bfhi(z[2])};
#pragma unroll
    for (int i = 0; i < 4; i++) { zz[i] += dpp<0xB1>(zz[i]); zz[i] += dpp<0x4E>(zz[i]); zz[i] += dpp<0x124>(zz[i]); zz[i] += dpp<0x128>(zz[i]); }
    if ((l & 15) == 0) *(LAS u32x2*)(size_t)(L_ZS + (l >> 4) * 32 + w * 8) = u32x2{pk(zz[0], zz[1]), pk(zz[2], zz[3])};
  }
  u32x4 xres[2];
#pragma unroll
  for (int i = 0; i < 2; i++) xres[i] = __builtin_amdgcn_raw_buffer_load_b128(__builtin_amdgcn_make_buffer_rsrc((void*)x, 0, 2u << 20, 0x00020000),
      (l >> 2) * 1024 + (l & 3) * 16, (tile2 * 32 + i * 16) * 1024 + n0 * 2, 0);
  BAR();
  {
    u32x4 k0 = *(LAS u32x4*)(size_t)(L_KVS + l * 32), k1 = *(LAS u32x4*)(size_t)(L_KVS + l * 32 + 16);
    u32x4 z0 = *(LAS u32x4*)(size_t)(L_ZS + (l >> 4) * 32), z1 = *(LAS u32x4*)(size_t)(L_ZS + (l >> 4) * 32 + 16);
    f32x16 o0 = mfma(r < 16 ? k1 : z1, qm0, mfma(r < 16 ? k0 : z0, qp0, Z));
    f32x16 o1 = mfma(r < 16 ? z1 : k1, qm1, mfma(r < 16 ? z0 : k0, qp1, Z));
    float d0 = __builtin_amdgcn_rcpf(o0[8] + 1e-6f), d1 = __builtin_amdgcn_rcpf(o1[0] + 1e-6f);
    o0 *= d0; o1 *= d1;
    __builtin_amdgcn_raw_buffer_store_b128(pk8(o0, 0), rs, l * 16, WS_O + (tile * 32 + 2 * m1) * 1024, SC1);
    __builtin_amdgcn_raw_buffer_store_b128(pk8(o1, 8), rs, l * 16, WS_O + (tile * 32 + 2 * m1 + 1) * 1024, SC1);
  }
  VMCNT(0);
  set_flag(rs, FL_O + tile * 16 + m1, tag, l);
  TS(11);
  float xv[16], gav[16], cbv[16];
#pragma unroll
  for (int i = 0; i < 2; i++) *(LAS u32x4*)(size_t)(R4 + w * 4096 + (i * 16 + (l >> 2)) * 80 + (l & 3) * 16) = xres[i];
#pragma unroll
  for (int i = 0; i < 4; i++) {
    u32x2 xx = *(LAS u32x2*)(size_t)(R4 + w * 4096 + r * 80 + i * 16 + h * 8);
    u32x2 ga = *(LAS u32x2*)(size_t)(L_MOD + (n0 + 8 * i + 4 * h) * 2), cb = *(LAS u32x2*)(size_t)(L_MOD + 2048 + (n0 + 8 * i + 4 * h) * 2);
#pragma unroll
    for (int q = 0; q < 4; q++) {
      xv[4 * i + q] = q & 1 ? bfhi(xx[q >> 1]) : bflo(xx[q >> 1]); gav[4 * i + q] = q & 1 ? bfhi(ga[q >> 1]) : bflo(ga[q >> 1]);
      cbv[4 * i + q] = q & 1 ? bfhi(cb[q >> 1]) : bflo(cb[q >> 1]);
      asm volatile("" : "+v"(xv[4 * i + q]), "+v"(gav[4 * i + q]), "+v"(cbv[4 * i + q]));
    }
  }
  BAR();
  unsigned x1[8];
  wait_flags(rs, FL_O + tile2 * 16 + kh * 8, 8, tag, l, budget);
  TS(12);
  {
#pragma unroll
    for (int s = 0; s < PRE; s++) af[s] = LDF(WS_O + (tile2 * 32 + kh * 16 + s) * 1024);
    f32x16 a0 = Z, a1 = Z;
    const unsigned b0 = trp + kh * (16384 + 32768), b1 = trp + kh * 16384 + (1 - kh) * 32768;
    gemm2<16, 1024, 512, 0>(b1, b0, af, a1, a0, [&](int k) { if (k + PRE < 16) af[k + PRE] = LDF(WS_O + (tile2 * 32 + kh * 16 + k + PRE) * 1024); });
#pragma unroll
    for (int i = 0; i < 4; i++) *(LAS f32x4*)(size_t)(R4 + (w ^ 2) * 4096 + i * 1024 + l * 16) = f32x4{a1[4 * i], a1[4 * i + 1], a1[4 * i + 2], a1[4 * i + 3]};
    TS(13);
    BAR();
    float s1 = 0, s2 = 0; f32x16 xs;
#pragma unroll
    for (int i = 0; i < 4; i++) {
      f32x4 o = *(LAS f32x4*)(size_t)(R4 + w * 4096 + i * 1024 + l * 16);
#pragma unroll
      for (int q = 0; q < 2; q++) {
        const int e = 4 * i + 2 * q;
        const unsigned v = pk(fmaf(gav[e], a0[e] + o[2 * q], xv[e]), fmaf(gav[e + 1], a0[e + 1] + o[2 * q + 1], xv[e + 1]));
        const float v0 = bflo(v), v1 = bfhi(v);
        x1[e >> 1] = v; s1 += v0 + v1; s2 = fmaf(v0, v0, fmaf(v1, v1, s2)); xs[e] = v0 * cbv[e]; xs[e + 1] = v1 * cbv[e + 1];
      }
    }
    __builtin_amdgcn_raw_buffer_store_b128(pk8(xs, 0), rs, l * 16, WS_XS + (tile2 * 32 + (n0 >> 4)) * 1024, SC1);
    __builtin_amdgcn_raw_buffer_store_b128(pk8(xs, 8), rs, l * 16, WS_XS + (tile2 * 32 + (n0 >> 4) + 1) * 1024, SC1);
    s1 = sum32(s1); s2 = sum32(s2);
    if (l < 32) __builtin_amdgcn_raw_buffer_store_b64(u32x2{__float_as_uint(s1), __float_as_uint(s2)}, rs, l * 8, WS_ST2 + (tile2 * 16 + (n0 >> 5)) * 256, SC1);
    VMCNT(0);
    set_flag(rs, FL_X + tile2 * 16 + (n0 >> 5), tag, l);
    if (b == 0 && t == 0) __builtin_amdgcn_raw_buffer_store_b32(tag, rs, WS_EPOCH, 0, SC1);
  }
  TS(14);
  if (w < 3 && (three || w < 2)) {
    u32x4 a;
    FENCE();
    for (;;) {
      a = __builtin_amdgcn_raw_buffer_load_b128(rs, WS_UVG + (h * 1376 + du_of(m3, w) * 32 + r) * 16, 0, SC1);
      if (__all(a[1] == tag && a[3] == tag) || budget == 0) break;
      budget--;
      FENCE();
    }
    const float sc = h ? 1.f : -1.44269504088896f;
    *(LAS f32x2*)(size_t)(L_UV + (w * 32 + r) * 16 + h * 8) = f32x2{__uint_as_float(a[0]) * sc, __uint_as_float(a[2]) * sc};
  }
  wait_flags(rs, FL_X + tile3 * 16, 16, tag, l, budget);
  TS(15);
  float mean2, rstd2;
  f32x16 ga0 = Z, u0 = Z, ga1 = Z, u1 = Z;
  {
    u32x2 st[8];
#pragma unroll
    for (int s = 0; s < PRE; s++) af[s] = LDF(WS_XS + (tile3 * 32 + s) * 1024);
    FENCE();
    gemm2<32, 1024, 512, 0>(R3 + trp, R2 + trp, af, ga0, u0, [&](int k) {
      if (!(k & 1) && (k >> 1) + PRE < 32) af[(k >> 1) + PRE] = LDF(WS_XS + (tile3 * 32 + (k >> 1) + PRE) * 1024);
      if (k >= 40 && k < 48) st[k - 40] = __builtin_amdgcn_raw_buffer_load_b64(rs, (h * 8 + k - 40) * 256 + r * 8, WS_ST2 + tile3 * 16 * 256, SC1);
    });
    float s1 = 0, s2 = 0;
#pragma unroll
    for (int i = 0; i < 8; i++) { s1 += __uint_as_float(st[i][0]); s2 += __uint_as_float(st[i][1]); }
    mean2 = sum32(s1) * (1.f / 512);
    rstd2 = __builtin_amdgcn_rsqf(fmaxf(sum32(s2) * (1.f / 512) - mean2 * mean2, 0.f) + 1e-5f);
  }
  const float nm = -mean2 * rstd2, rl = -1.44269504088896f * rstd2;
  const unsigned uvb = opq(L_UV + 64 * h);
  f32x4 uv[2]; float eg, eu; unsigned hv[8]; float hlo;
  auto ep = [&](int k, int p, const f32x16& ag, const f32x16& au, bool pin) {
    if (k >= 51) return;
    if (k % 3 == 0 && k < 48) uv[(k / 3) & 1] = *(LAS f32x4*)(size_t)(uvb + (p * 32 + 8 * (k / 12) + (k / 3 & 3)) * 16);
    if (k < 3) return;
    const int e = k / 3 - 1, c = k % 3;
    const f32x4 d = uv[e & 1];
    if (c == 0) { eg = __builtin_amdgcn_exp2f(fmaf(ag[e], rl, fmaf(nm, d[0], d[1]))); if (pin) asm volatile("" : "+v"(eg)); }
    if (c == 1) { eg = __builtin_amdgcn_rcpf(1.f + eg); eu = fmaf(nm, d[2], d[3]); if (pin) asm volatile("" : "+v"(eg), "+v"(eu)); }
    if (c == 2) {
      float v = fmaf(au[e], rstd2, eu) * eg;
      if (e & 1) hv[e >> 1] = pin ? opq(pk(hlo, v)) : pk(hlo, v); else { hlo = v; if (pin) asm volatile("" : "+v"(hlo)); }
      if ((e & 7) == 7) __builtin_amdgcn_raw_buffer_store_b128(u32x4{hv[(e >> 1) - 3], hv[(e >> 1) - 2], hv[(e >> 1) - 1], hv[e >> 1]}, rs, l * 16,
                                                               WS_HID + (tile3 * 86 + du_of(m3, p) * 2 + (e >> 3)) * 1024, SC1);
    }
  };
  {
    TS(16);
    BAR();
    gemm2<32, 1024, 512, 0>(R0 + trp, R1 + trp, af, ga1, u1, [&](int k) { ep(k, 0, ga0, u0, true); });
    TS(17);
    BAR();
    const unsigned hb = WS_HID + (tile2 * 86 + kh) * 1024;
    const unsigned fd = WS_FLG + (FL_D + tile2 * 48 + (l & 15)) * 64;
    unsigned fv = 0;
    if (three) {
      ga0 = Z; u0 = Z;
      gemm2<32, 1024, 512, 0>(R3 + trp, R2 + trp, af, ga0, u0, [&](int k) {
        if (k == 10) { VMCNT(0); set_flag(rs, FL_D + tile3 * 48 + m3, tag, l); }
        ep(k, 1, ga1, u1, true);
        if (k == 56) { fv = __builtin_amdgcn_raw_buffer_load_b32(rs, fd, 0, SC1); FENCE(); }
      });
      ga1 = ga0; u1 = u0;
    }
    TS(18);
    BAR();
    const int pl = three ? 2 : 1;
#pragma unroll
    for (int k = 0; k < 24; k++) ep(k, pl, ga1, u1, false);
    asm volatile("" : "+v"(hv[0]), "+v"(hv[1]), "+v"(hv[2]));
    VMCNT(0);
    set_flag(rs, FL_D + tile3 * 48 + (pl - 1) * 16 + m3, tag, l);
    const bool early = three && __all(fv == tag);
    if (early) {
#pragma unroll
      for (int s = 0; s < 8; s++) af[s] = LDF(hb + du_of(s, 0) * 2048);
    }
    FENCE();
#pragma unroll
    for (int k = 24; k < 64; k++) ep(k, pl, ga1, u1, false);
    FENCE();
    if (!early) {
      if (!three) { VMCNT(0); set_flag(rs, FL_D + tile3 * 48 + 16 + m3, tag, l); }
      wait_flags(rs, FL_D + tile2 * 48, 16, tag, l, budget);
#pragma unroll
      for (int s = 0; s < 8; s++) af[s] = LDF(hb + du_of(s, 0) * 2048);
    }
    TS(19);
    f32x16 a0 = Z, a1 = Z;
    const unsigned b0 = trp + kh * 2048 + kh * 512, b1 = trp + kh * 2048 + (1 - kh) * 512;
    gemm2<16, 4096, 1024, 0>(R0 + b1, R0 + b0, af, a1, a0, [&](int k) {
      if (k < 8) af[8 + k] = LDF(hb + du_of(8 + k, 0) * 2048);
      if (k == KF) {
        VMCNT(0);
        if (three) set_flag(rs, FL_D + tile3 * 48 + 32 + m3, tag, l);
        fv = __builtin_amdgcn_raw_buffer_load_b32(rs, fd + 16 * 64, 0, SC1); FENCE();
      }
    });
    TS(20);
    if (!__all(fv == tag)) wait_flags(rs, FL_D + tile2 * 48 + 16, 16, tag, l, budget);
    FENCE();
#pragma unroll
    for (int s = 0; s < 12; s++) af[16 + s] = LDF(hb + du_of(s, 1) * 2048);
    BAR();
    TS(21);
    gemm2<16, 4096, 1024, 0>(R2 + b1, R2 + b0, af + 16, a1, a0, [&](int k) {
      if (k < 4) af[28 + k] = LDF(hb + du_of(12 + k, 1) * 2048);
      if (k == 4) { fv = __builtin_amdgcn_raw_buffer_load_b32(rs, WS_FLG + (FL_D + tile2 * 48 + 32 + (l < 11 ? l : 0)) * 64, 0, SC1); FENCE(); }
      if (k == 20) { if (!__all(fv == tag)) wait_flags(rs, FL_D + tile2 * 48 + 32, 11, tag, l, budget); FENCE(); }
      if (k >= 20 && k < 31) af[k - 20] = LDF(hb + du_of(k - 20, 2) * 2048);
    });
    TS(22);
    BAR();
    gemm2<6, 4096, 1024, 0>(R4 + b1, R4 + b0, af, a1, a0, NoFill());
    gemm2<5, 4096, 1024, 0>(R0 + b1, R0 + b0, af + 6, a1, a0, NoFill());
    const int l2 = opq(l);
#pragma unroll
    for (int i = 0; i < 4; i++) *(LAS f32x4*)(size_t)(R2 + (w ^ 2) * 4096 + i * 1024 + l2 * 16) = f32x4{a1[4 * i], a1[4 * i + 1], a1[4 * i + 2], a1[4 * i + 3]};
    TS(23);
    BAR();
#pragma unroll
    for (int i = 0; i < 4; i++) {
      f32x4 o = *(LAS f32x4*)(size_t)(R2 + w * 4096 + i * 1024 + l2 * 16);
      u32x2 gb = *(LAS u32x2*)(size_t)(L_MOD + 3072 + (n0 + 8 * i + 4 * (l2 >> 5)) * 2);
      u32x2 y = {pk(fmaf(bflo(gb[0]), a0[4 * i] + o[0], bflo(x1[2 * i])), fmaf(bfhi(gb[0]), a0[4 * i + 1] + o[1], bfhi(x1[2 * i]))),
                 pk(fmaf(bflo(gb[1]), a0[4 * i + 2] + o[2], bflo(x1[2 * i + 1])), fmaf(bfhi(gb[1]), a0[4 * i + 3] + o[3], bfhi(x1[2 * i + 1])))};
      *(LAS u32x2*)(size_t)(R3 + w * 4096 + (l2 & 31) * 80 + i * 16 + (l2 >> 5) * 8) = y;
    }
#pragma unroll
    for (int i = 0; i < 2; i++)
      __builtin_amdgcn_raw_buffer_store_b128(*(LAS u32x4*)(size_t)(R3 + w * 4096 + (i * 16 + (l2 >> 2)) * 80 + (l2 & 3) * 16), __builtin_amdgcn_make_buffer_rsrc(out, 0, 2u << 20, 0x00020000),
                                             (l2 >> 2) * 1024 + (l2 & 3) * 16, (tile2 * 32 + i * 16) * 1024 + n0 * 2, SC1);
  }
  TS(24); TS(25);
}

extern "C" void kernel_launch(void* const* d_in, const int* in_sizes, int n_in, void* d_out, int out_size, void* d_ws, size_t ws_size,
                              hipStream_t stream) {
  (void)hipFuncSetAttribute((const void*)fused_block, hipFuncAttributeMaxDynamicSharedMemorySize, 163840);
  fused_block<<<256, 320, 163840, stream>>>((const u16*)d_in[0], (const u16*)d_in[1], (const u16*)d_in[2], (const u16*)d_in[3], (const u16*)d_in[5],
                                            (const u16*)d_in[6], (const u16*)d_in[7], (const u16*)d_in[8], (const u16*)d_in[9], (u16*)d_out, d_ws);
}
```

```cpp
#include <hip/hip_runtime.h>
#include <hip/hip_bf16.h>

typedef __bf16 bf16x8 __attribute__((ext_vector_type(8)));
typedef __bf16 bf16x2 __attribute__((ext_vector_type(2)));
typedef short s16x4 __attribute__((ext_vector_type(4)));
typedef float f32x16 __attribute__((ext_vector_type(16)));
typedef float f32x4 __attribute__((ext_vector_type(4)));
typedef float f32x2 __attribute__((ext_vector_type(2)));
typedef unsigned u32x4 __attribute__((ext_vector_type(4)));
typedef unsigned u32x2 __attribute__((ext_vector_type(2)));
typedef unsigned short u16;
typedef _Float16 f16x2 __attribute__((ext_vector_type(2)));
#define LAS __attribute__((address_space(3)))
#define DEV __device__ __forceinline__

constexpr unsigned WS_EPOCH = 0;
constexpr unsigned WS_MODG = 4096;
constexpr unsigned WS_UVG  = WS_MODG + 40960;
constexpr unsigned WS_FLG  = WS_UVG + 45056;
constexpr unsigned FL_H = 0;
constexpr unsigned FL_O = FL_H + 32 * 32;
constexpr unsigned FL_X = FL_O + 64 * 16;
constexpr unsigned FL_D = FL_X + 64 * 16;
constexpr unsigned FL_N = FL_D + 64 * 48;
constexpr unsigned WS_KVP  = WS_FLG + FL_N * 64;
constexpr unsigned WS_ST2  = WS_KVP + 256 * 4352;
constexpr unsigned WS_H1   = WS_ST2 + 64 * 16 * 256;
constexpr unsigned WS_O    = WS_H1 + (2u << 20);
constexpr unsigned WS_XS   = WS_O + (2u << 20);
constexpr unsigned WS_HID  = WS_XS + (2u << 20);
constexpr unsigned WS_END  = WS_HID + 64 * 86 * 1024;
constexpr unsigned WS_DBG  = 32u << 20;
constexpr unsigned R0 = 0, R1 = 32768, R2 = 65536, R3 = 98304, R4 = 131072;
constexpr unsigned L_MOD = 155648;
constexpr unsigned L_UV = L_MOD + 4096;
constexpr unsigned L_RED = L_UV + 1536;
constexpr unsigned L_END = L_RED + 2048;
constexpr unsigned L_KVS = R4 + 18432, L_ZS = L_KVS + 2048, L_PHI = L_ZS + 1024;
static_assert(L_END <= 163840, "lds");

DEV float bflo(unsigned u) { return __uint_as_float(u << 16); }
DEV float bfhi(unsigned u) { return __uint_as_float(u & 0xffff0000u); }
DEV unsigned pk(float a, float b) { bf16x2 v = {(__bf16)a, (__bf16)b}; return __builtin_bit_cast(unsigned, v); }
DEV float rbf(float a) { return (float)(__bf16)a; }
DEV f32x16 mfma(u32x4 a, u32x4 b, f32x16 c) {
  return __builtin_amdgcn_mfma_f32_32x32x16_bf16(__builtin_bit_cast(bf16x8, a), __builtin_bit_cast(bf16x8, b), c, 0, 0, 0);
}
DEV u32x4 pk8(const f32x16& a, int o) {
  return u32x4{pk(a[o], a[o + 1]), pk(a[o + 2], a[o + 3]), pk(a[o + 4], a[o + 5]), pk(a[o + 6], a[o + 7])};
}
template <int ESTEP> DEV u32x4 wfrag(unsigned addr) {
  s16x4 a = __builtin_amdgcn_ds_read_tr16_b64_v4i16((LAS s16x4*)(size_t)addr);
  s16x4 b = __builtin_amdgcn_ds_read_tr16_b64_v4i16((LAS s16x4*)(size_t)(addr + ESTEP));
  u32x2 x = __builtin_bit_cast(u32x2, a), y = __builtin_bit_cast(u32x2, b);
  return u32x4{x[0], x[1], y[0], y[1]};
}
DEV unsigned opq(unsigned v) { asm volatile("" : "+v"(v)); return v; }
struct NoFill { DEV void operator()(int) const {} };
template <int N, int STEP, int ESTEP, int MODE, class F>
DEV void gemm2(unsigned ba, unsigned bb, u32x4* af, f32x16& ca, f32x16& cb, F fill) {
  ba = opq(ba); bb = opq(bb);
  constexpr int D = MODE == 2 ? 4 : 2;
  u32x4 fa[D + 1], fb[D + 1];
#pragma unroll
  for (int s = 0; s < D; s++) { fa[s] = wfrag<ESTEP>(ba + s * STEP); if (MODE != 2) fb[s] = wfrag<ESTEP>(bb + s * STEP); }
#pragma unroll
  for (int s = 0; s < N; s++) {
    __builtin_amdgcn_sched_barrier(0);
    ca = mfma(fa[s % (D + 1)], af[s], ca);
    asm volatile("" : "+v"(ca));
    if (s + D < N) fa[(s + D) % (D + 1)] = wfrag<ESTEP>(ba + (s + D) * STEP);
    fill(2 * s);
    if (MODE != 2) {
      __builtin_amdgcn_sched_barrier(0);
      cb = MODE == 1 ? mfma(af[s], fb[s % (D + 1)], cb) : mfma(fb[s % (D + 1)], af[s], cb);
      asm volatile("" : "+v"(cb));
      if (s + D < N) fb[(s + D) % (D + 1)] = wfrag<ESTEP>(bb + (s + D) * STEP);
      fill(2 * s + 1);
    }
  }
  __builtin_amdgcn_sched_barrier(0);
}
template <int CTRL> DEV float dpp(float v) {
  return __int_as_float(__builtin_amdgcn_update_dpp(0, __float_as_int(v), CTRL, 0xf, 0xf, true));
}
DEV float sum32(float v) { u32x2 r = __builtin_amdgcn_permlane32_swap(__float_as_uint(v), __float_as_uint(v), false, false); return __uint_as_float(r[0]) + __uint_as_float(r[1]); }
DEV float sum16(float v) { u32x2 r = __builtin_amdgcn_permlane16_swap(__float_as_uint(v), __float_as_uint(v), false, false); return __uint_as_float(r[0]) + __uint_as_float(r[1]); }
DEV float max32(float v) { u32x2 r = __builtin_amdgcn_permlane32_swap(__float_as_uint(v), __float_as_uint(v), false, false); return fmaxf(__uint_as_float(r[0]), __uint_as_float(r[1])); }
DEV float wavesum(float v) { v += dpp<0xB1>(v); v += dpp<0x4E>(v); v += dpp<0x124>(v); v += dpp<0x128>(v); return sum32(sum16(v)); }
#define DMAI "global_load_lds_dwordx4 %1, %2\n\t"
template <int VSTEP, int LSTEP, int PACE> DEV void dman(const void* sbase, unsigned voff, unsigned lds, int n) {
  unsigned keep; lds = __builtin_amdgcn_readfirstlane(lds);
  asm volatile("s_mov_b32 %0, m0\n\ts_mov_b32 m0, %3\n\ts_nop 2\n"
               "1:\n\ts_nop %c7\n\ts_waitcnt vmcnt(60)\n\t" DMAI "v_add_u32 %1, %5, %1\n\ts_add_u32 m0, m0, %6\n\ts_sub_u32 %4, %4, 1\n\ts_cbranch_scc0 1b\n\ts_mov_b32 m0, %0"
               : "=&s"(keep), "+v"(voff), "+s"(sbase), "+s"(lds), "+s"(n) : "i"(VSTEP), "i"(LSTEP), "i"(PACE) : "memory", "scc");
}
#define FASTP 99
#define DMA4(v) DMAI "s_add_u32 m0, m0, 0x400\n\tv_add_u32 %1, " v ", %1\n\ts_nop %c7\n\t"
template <int V1, int V4, int PACE> DEV void dma4(const void* sbase, unsigned voff, unsigned lds, int n) {
  unsigned keep; lds = __builtin_amdgcn_readfirstlane(lds);
  asm volatile("s_mov_b32 %0, m0\n\ts_mov_b32 m0, %3\n\ts_nop 2\n"
               "1:\n\ts_waitcnt vmcnt(56)\n\t" DMA4("%5") DMA4("%5") DMA4("%5") DMA4("%6") "s_sub_u32 %4, %4, 1\n\ts_cbranch_scc0 1b\n\ts_mov_b32 m0, %0"
               : "=&s"(keep), "+v"(voff), "+s"(sbase), "+s"(lds), "+s"(n) : "i"(V1), "i"(V4), "i"(PACE) : "memory", "scc");
}
template <int PITCH, int PACE> DEV void dma_tile(const u16* src, int col, unsigned lds, int l) {
  if (PACE < FASTP) dma4<16 * PITCH, 16 * PITCH, PACE>(src, (l >> 2) * PITCH + col * 2 + (l & 3) * 16, lds, 7);
  else dman<16 * PITCH, 1024, PACE>(src, (l >> 2) * PITCH + col * 2 + (l & 3) * 16, lds, 31);
}
DEV int du_of(int m, int p) { return m < 11 ? 3 * m + p : 11 + 2 * m + p; }
template <int PACE> DEV void dma_wd(const u16* src, int ct, int p, int m0, int m1, unsigned lds, int l) {
  const unsigned voff = (du_of(m0, p) * 32 + ((l >> 2) & 7)) * 1024 + ct * 128 + (l >> 5) * 64 + (l & 3) * 16;
  if (m0 < 11) dma4<8192, 3 * 32768 - 3 * 8192, PACE>(src, voff, lds, m1 - m0 - 1);
  else dma4<8192, 2 * 32768 - 3 * 8192, PACE>(src, voff, lds, m1 - m0 - 1);
}
#define VMCNT(n) asm volatile("s_waitcnt vmcnt(" #n ")" ::: "memory")
#define BAR() asm volatile("s_waitcnt lgkmcnt(0)\n\ts_barrier" ::: "memory")
#define FENCE() asm volatile("" ::: "memory")
constexpr int SC1 = 16;
#define PA 8
#define PB 2
#define KF 18
#define PC 0
#define PD 0
#define PW 3
#define PREN 12
constexpr int PRE = PREN;
#define LDF(base) __builtin_amdgcn_raw_buffer_load_b128(rs, l * 16, (base), SC1)
constexpr unsigned SPIN_MAX = 1u << 18;
#define TS(i)

DEV void wait_flags(__amdgpu_buffer_rsrc_t rs, unsigned first, int n, unsigned tag, int l, unsigned& budget) {
  unsigned off = WS_FLG + (first + (l < n ? l : 0)) * 64;
  FENCE();
  for (;;) {
    unsigned v = __builtin_amdgcn_raw_buffer_load_b32(rs, off, 0, SC1);
    if (__all(v == tag) || budget == 0) break;
    budget--;
    FENCE();
  }
  FENCE();
}
DEV void set_flag(__amdgpu_buffer_rsrc_t rs, unsigned idx, unsigned tag, int l) {
  if (l == 0) __builtin_amdgcn_raw_buffer_store_b32(tag, rs, WS_FLG + idx * 64, 0, SC1);
  FENCE();
}
DEV void gemv_red(float (&a)[8], unsigned col0, int t) {
#pragma unroll
  for (int j = 0; j < 8; j++) { a[j] += dpp<0x4E>(a[j]); a[j] += dpp<0x124>(a[j]); a[j] += dpp<0x128>(a[j]); }
  if ((t & 15) < 2) {
    LAS f32x4* p = (LAS f32x4*)(size_t)(L_RED + ((t >> 4) * 32 + col0 + (t & 1) * 8) * 4);
    p[0] = f32x4{a[0], a[1], a[2], a[3]}; p[1] = f32x4{a[4], a[5], a[6], a[7]};
  }
}
DEV void gemv_acc(float (&a)[8], u32x4 v, float s) {
#pragma unroll
  for (int j = 0; j < 4; j++) { a[2 * j] += s * bflo(v[j]); a[2 * j + 1] += s * bfhi(v[j]); }
}
DEV float red_col(int col, int half) {
  float s = 0;
#pragma unroll
  for (int r = 0; r < 8; r++) s += *(LAS float*)(size_t)(L_RED + ((half * 8 + r) * 32 + col) * 4);
  return sum32(s);
}
DEV void phi(const f32x16& x, int o, u32x4& pp, u32x4& pm) {
  float mx = x[o], mn = x[o];
#pragma unroll
  for (int j = 1; j < 8; j++) { mx = fmaxf(mx, x[o + j]); mn = fminf(mn, x[o + j]); }
  mx = max32(mx); mn = -max32(-mn);
  const float L = 1.44269504088896f;
  float ep[8], em[8], sp = 0, sm = 0, a = -mx * L, c = mn * L;
#pragma unroll
  for (int j = 0; j < 8; j++) {
    ep[j] = __builtin_amdgcn_exp2f(fmaf(x[o + j], L, a)); em[j] = __builtin_amdgcn_exp2f(fmaf(x[o + j], -L, c));
    sp += ep[j]; sm += em[j];
  }
  sp = __builtin_amdgcn_rcpf(sum32(sp)); sm = __builtin_amdgcn_rcpf(sum32(sm));
#pragma unroll
  for (int j = 0; j < 4; j++) { pp[j] = pk(ep[2 * j] * sp, ep[2 * j + 1] * sp); pm[j] = pk(em[2 * j] * sm, em[2 * j + 1] * sm); }
}

__global__ __launch_bounds__(320) void fused_block(const u16* __restrict__ x, const u16* __restrict__ wqkv, const u16* __restrict__ wphi,
    const u16* __restrict__ wo, const u16* __restrict__ wg, const u16* __restrict__ wu, const u16* __restrict__ wd,
    const u16* __restrict__ cvec, const u16* __restrict__ wmod, u16* __restrict__ out, void* ws) {
  extern __shared__ __attribute__((aligned(16))) char smem[];
  const int t = threadIdx.x, l = t & 63, w = __builtin_amdgcn_readfirstlane(t >> 6), b = blockIdx.x;
  const int xc = b & 7, j = b >> 3, g = xc * 2 + (j >> 4), m = j & 15, h = l >> 5, r = l & 31;
  const int th = m >> 3, ct = m & 7, tt = w & 1, kh = w >> 1;
  const __amdgpu_buffer_rsrc_t rs = __builtin_amdgcn_make_buffer_rsrc(ws, 0, WS_END, 0x00020000);
  unsigned budget = SPIN_MAX;
  const f32x16 Z = {0, 0, 0, 0, 0, 0, 0, 0, 0, 0, 0, 0, 0, 0, 0, 0};
  TS(0); TS(1);
  const int m1 = (xc & 3) * 4 + (j & 3), g1 = (xc >> 2) * 8 + (j >> 2), m3 = (xc & 1) * 8 + (j & 7), g3 = (xc >> 1) * 4 + (j >> 3);
  const bool three = m3 < 11;
  if (w == 4) {
    const unsigned ltag = __builtin_amdgcn_raw_buffer_load_b32(rs, WS_EPOCH, 0, SC1) + 1;
    const int iu = xc * 22 + j;
    const bool do_uv = j < 22 && iu < 172;
    u32x4 wuv[16];
    unsigned touch;
    {
      const int id = l < 12 ? l >> 1 : 5, idx = j * 2 + (l & 1);
      const size_t base = id == 0 ? (size_t)ws : id == 1 ? (size_t)out : id == 2 ? (size_t)wo : id == 3 ? (size_t)wg : id == 4 ? (size_t)wu : (size_t)wd;
      const unsigned step = id == 0 ? WS_END / 64 & ~63u : id == 1 ? 32768 : id == 2 ? 8192 : 22016;
      touch = __hip_atomic_load((const __attribute__((address_space(1))) unsigned*)(base + idx * step), __ATOMIC_RELAXED, __HIP_MEMORY_SCOPE_AGENT);
    }
    __builtin_amdgcn_s_sleep(4);
    __builtin_amdgcn_s_setprio(3);
    dman<0, 0, 0>(wphi, m1 * 1024 + l * 16, L_PHI, 0);
    dma_tile<3072, PA>(wqkv, 512 + m1 * 32, R0, l);
    BAR(); BAR();
    dma_tile<3072, PA>(wqkv, 1024 + m1 * 32, R1, l);
    VMCNT(0); BAR();
    dma_tile<3072, PA>(wqkv, m1 * 32, R2, l);
    asm volatile("" :: "v"(ltag), "v"(touch));
    if (do_uv) {
      const __amdgpu_buffer_rsrc_t rw = __builtin_amdgcn_make_buffer_rsrc((void*)(iu < 86 ? wg : wu), 0, 512 * 2752, 0x00020000);
#pragma unroll
      for (int i = 0; i < 16; i++) { wuv[i] = __builtin_amdgcn_raw_buffer_load_b128(rw, (l >> 1) * 16 * 2752 + (l & 1) * 16, (iu < 86 ? iu : iu - 86) * 32 + i * 2752, 0); asm volatile("s_sleep 2" ::: "memory"); }
    }
    dma_tile<2752, PA>(wg, du_of(m3, 0) * 32, R3, l);
    unsigned wt[6];
    auto warm = [&]() {
#pragma unroll
      for (int i = 0; i < 6; i++) wt[i] = __builtin_amdgcn_raw_buffer_load_b32(__builtin_amdgcn_make_buffer_rsrc((void*)wd, 0, 1376 * 1024, 0x00020000), (j * 344 + i * 64 + l) * 128, 0, 0);
      FENCE();
    };
    VMCNT(32);
    if (PW == 4) warm();
    BAR();
    dma_tile<1024, PB>(wo, ct * 64, R0, l); dma_tile<1024, PB>(wo, ct * 64 + 32, R1, l);
    BAR();
    dma_tile<2752, PB>(wu, du_of(m3, 0) * 32, R2, l);
    VMCNT(32); BAR();
    __builtin_amdgcn_s_setprio(0);
    if (do_uv) {
      float a[8] = {0, 0, 0, 0, 0, 0, 0, 0}, c[8] = {0, 0, 0, 0, 0, 0, 0, 0};
#pragma unroll
      for (int i = 0; i < 2; i++) {
        const u32x4 c1 = *(LAS u32x4*)(size_t)(L_MOD + 2048 + (l >> 1) * 32 + i * 16), sb = *(LAS u32x4*)(size_t)(L_MOD + 1024 + (l >> 1) * 32 + i * 16);
#pragma unroll
        for (int q = 0; q < 8; q++) {
          gemv_acc(a, wuv[8 * i + q], q & 1 ? bfhi(c1[q >> 1]) : bflo(c1[q >> 1]));
          gemv_acc(c, wuv[8 * i + q], q & 1 ? bfhi(sb[q >> 1]) : bflo(sb[q >> 1]));
        }
      }
#pragma unroll
      for (int q = 0; q < 8; q++) {
        a[q] += dpp<0x4E>(a[q]); a[q] += dpp<0x124>(a[q]); a[q] += dpp<0x128>(a[q]); a[q] = sum32(sum16(a[q]));
        c[q] += dpp<0x4E>(c[q]); c[q] += dpp<0x124>(c[q]); c[q] += dpp<0x128>(c[q]); c[q] = sum32(sum16(c[q]));
      }
      float ua = a[0], va = c[0];
#pragma unroll
      for (int q = 1; q < 8; q++) { ua = (l >> 1) == q ? a[q] : ua; va = (l >> 1) == q ? c[q] : va; }
      VMCNT(0);
      if (l < 16) __builtin_amdgcn_raw_buffer_store_b128(u32x4{__float_as_uint(ua), ltag, __float_as_uint(va), ltag}, rs, WS_UVG + (iu * 16 + (l & 1) * 8 + (l >> 1)) * 16, 0, SC1);
    }
    else VMCNT(0);
    __builtin_amdgcn_s_setprio(3);
    BAR();
    dma_tile<2752, PA>(wg, du_of(m3, 1) * 32, R0, l); dma_tile<2752, PA>(wu, du_of(m3, 1) * 32, R1, l);
    if (PW == 1) warm();
    VMCNT(0); BAR();
    if (PW == 2) warm();
    if (three) { dma_tile<2752, 0>(wg, du_of(m3, 2) * 32, R3, l); dma_tile<2752, 0>(wu, du_of(m3, 2) * 32, R2, l); }
    VMCNT(0);
    if (PW == 3) warm();
    BAR();
    dma_wd<PC>(wd, ct, 0, 0, 11, R0, l); dma_wd<PC>(wd, ct, 0, 11, 16, R0 + 11 * 4096, l);
    dma_wd<0>(wd, ct, 2, 0, 6, R4, l);
    VMCNT(24); BAR();
    dma_wd<PD>(wd, ct, 1, 0, 11, R2, l); dma_wd<PD>(wd, ct, 1, 11, 16, R2 + 11 * 4096, l);
    VMCNT(0);
    if (PW) asm volatile("" :: "v"(wt[0]), "v"(wt[1]), "v"(wt[2]), "v"(wt[3]), "v"(wt[4]), "v"(wt[5]));
    BAR();
    dma_wd<0>(wd, ct, 2, 6, 11, R0, l);
    VMCNT(0); BAR();
    BAR();
    return;
  }
  const int grow = (t >> 1) * 4, gcol = (t & 1) * 8, im = xc * 24 + j;
  const int srow = g * 128 + m * 8 + w * 2;
  u32x4 wa[4]; u32x2 cv;
  const u32x4 xr0 = *(const u32x4*)(x + (size_t)srow * 512 + l * 8), xr1 = *(const u32x4*)(x + (size_t)(srow + 1) * 512 + l * 8);
  const unsigned tagv = __builtin_amdgcn_raw_buffer_load_b32(rs, WS_EPOCH, 0, SC1);
  if (j < 24) {
    cv = *(const u32x2*)(cvec + grow);
#pragma unroll
    for (int i = 0; i < 4; i++) wa[i] = *(const u32x4*)(wmod + (size_t)(grow + i) * 3072 + im * 16 + gcol);
  }
  FENCE();
  {
    float v0[8], v1[8], s0 = 0, s1 = 0;
#pragma unroll
    for (int i = 0; i < 4; i++) { v0[2 * i] = bflo(xr0[i]); v0[2 * i + 1] = bfhi(xr0[i]); v1[2 * i] = bflo(xr1[i]); v1[2 * i + 1] = bfhi(xr1[i]); }
#pragma unroll
    for (int i = 0; i < 8; i++) { s0 += v0[i]; s1 += v1[i]; }
    s0 = wavesum(s0) * (1.f / 512); s1 = wavesum(s1) * (1.f / 512);
    float q0 = 0, q1 = 0;
#pragma unroll
    for (int i = 0; i < 8; i++) { v0[i] -= s0; v1[i] -= s1; q0 += v0[i] * v0[i]; q1 += v1[i] * v1[i]; }
    q0 = __builtin_amdgcn_rsqf(wavesum(q0) * (1.f / 512) + 1e-5f); q1 = __builtin_amdgcn_rsqf(wavesum(q1) * (1.f / 512) + 1e-5f);
    u32x4 y0, y1;
#pragma unroll
    for (int i = 0; i < 4; i++) {
      y0[i] = __builtin_bit_cast(unsigned, f16x2{(_Float16)(v0[2 * i] * q0), (_Float16)(v0[2 * i + 1] * q0)});
      y1[i] = __builtin_bit_cast(unsigned, f16x2{(_Float16)(v1[2 * i] * q1), (_Float16)(v1[2 * i + 1] * q1)});
    }
    *(LAS u32x4*)(size_t)(R4 + (w * 2) * 1040 + l * 16) = y0;
    *(LAS u32x4*)(size_t)(R4 + (w * 2 + 1) * 1040 + l * 16) = y1;
  }
  const unsigned tag = __builtin_amdgcn_readfirstlane(tagv) + 1;
  BAR();
  TS(2);
#pragma unroll
  for (int i = 0; i < 2; i++) {
    const int c = w * 16 + i * 8 + (l >> 3);
    __builtin_amdgcn_raw_buffer_store_b128(*(LAS u32x4*)(size_t)(R4 + (l & 7) * 1040 + c * 16), rs, c * 512 + ((m & 3) * 8 + (l & 7)) * 16,
                                           WS_H1 + (g * 4 + (m >> 2)) * 32768, SC1);
  }
  FENCE();
  if (j < 24) {
    float a[8] = {0, 0, 0, 0, 0, 0, 0, 0};
#pragma unroll
    for (int i = 0; i < 4; i++) {
      float c = i & 1 ? bfhi(cv[i >> 1]) : bflo(cv[i >> 1]);
      gemv_acc(a, wa[i], rbf(c * __builtin_amdgcn_rcpf(1.f + __builtin_amdgcn_exp2f(-1.44269504088896f * c))));
    }
    gemv_red(a, 0, t);
  }
  BAR();
  TS(3);
  VMCNT(0);
  if (j < 24 && w == 0) {
    float v = red_col(r & 15, h), v1 = dpp<0xB1>(v);
    if (l < 16 && !(l & 1)) __builtin_amdgcn_raw_buffer_store_b64(u32x2{pk(v, v1), tag}, rs, WS_MODG + (im * 8 + (l >> 1)) * 8, 0, SC1);
  }
  set_flag(rs, FL_H + (g * 4 + (m >> 2)) * 16 + (m & 3) * 4 + w, tag, l);
  TS(4);
  const int tile = g1 * 4 + w, tile3 = g3 * 4 + w;
  const int tile2 = g * 4 + th * 2 + tt;
  const int n0 = ct * 64 + kh * 32;
  const unsigned trb = ((l & 15) >> 2) * 64 + ((l >> 4) & 1) * 32 + (l & 3) * 8;
  const unsigned trn = trb + h * 512, trp = trb + h * 256;
  u32x4 af[32];
  {
    u32x4 a; unsigned f;
    const unsigned fo = WS_FLG + (FL_H + tile * 16 + (l & 15)) * 64;
    FENCE();
    for (;;) {
      a = __builtin_amdgcn_raw_buffer_load_b128(rs, WS_MODG + t * 16, 0, SC1);
      f = __builtin_amdgcn_raw_buffer_load_b32(rs, fo, 0, SC1);
      if (__all(a[1] == tag && a[3] == tag && f == tag) || budget == 0) break;
      budget--;
      FENCE();
    }
    FENCE();
    TS(5);
#pragma unroll
    for (int s = 0; s < PRE; s++) af[s] = LDF(WS_H1 + (tile * 32 + s) * 1024);
    f32x4 v = {bflo(a[0]), bfhi(a[0]), bflo(a[2]), bfhi(a[2])};
    if (w >= 2) v = f32x4{rbf(1.f + v[0]), rbf(1.f + v[1]), rbf(1.f + v[2]), rbf(1.f + v[3])};
    *(LAS f32x4*)(size_t)(L_MOD + t * 16) = v;
  }
  BAR();
  TS(6);
  u32x4 ph0, ph1, idp, idm;
  {
    f32x16 ak = Z, av = Z;
    const unsigned mb = opq(L_MOD + h * 32);
    f32x4 ms[3][2];
    auto ldm = [&](int u) {
      ms[u % 3][0] = *(LAS f32x4*)(size_t)(mb + (u >> 1) * 64 + (u & 1) * 16); ms[u % 3][1] = *(LAS f32x4*)(size_t)(mb + (u >> 1) * 64 + (u & 1) * 16 + 2048);
    };
    auto mod = [&](int u) {
      const unsigned ya = af[u >> 1][2 * (u & 1)], yb = af[u >> 1][2 * (u & 1) + 1]; unsigned oa, ob; float t0, t1, t2, t3;
      const f32x4 c = ms[u % 3][1], d = ms[u % 3][0];
      asm volatile("v_fma_mix_f32 %2, %6, %8, %12 op_sel_hi:[1,0,0]\n\tv_fma_mix_f32 %3, %6, %9, %13 op_sel:[1,0,0] op_sel_hi:[1,0,0]\n\t"
                   "v_fma_mix_f32 %4, %7, %10, %14 op_sel_hi:[1,0,0]\n\tv_fma_mix_f32 %5, %7, %11, %15 op_sel:[1,0,0] op_sel_hi:[1,0,0]\n\t"
                   "v_cvt_pk_bf16_f32 %0, %2, %3\n\tv_cvt_pk_bf16_f32 %1, %4, %5"
                   : "=v"(oa), "=v"(ob), "=&v"(t0), "=&v"(t1), "=&v"(t2), "=&v"(t3)
                   : "v"(ya), "v"(yb), "v"(c[0]), "v"(c[1]), "v"(c[2]), "v"(c[3]), "v"(d[0]), "v"(d[1]), "v"(d[2]), "v"(d[3]));
      af[u >> 1][2 * (u & 1)] = oa; af[u >> 1][2 * (u & 1) + 1] = ob;
    };
    ldm(0); ldm(1); ldm(2); mod(0); ldm(3); mod(1); ldm(4); mod(2);
    gemm2<32, 1024, 256, 1>(R0 + trn, R1 + trn, af, ak, av, [&](int k) {
      if (!(k & 1) && (k >> 1) + PRE < 32) af[(k >> 1) + PRE] = LDF(WS_H1 + (tile * 32 + (k >> 1) + PRE) * 1024);
      if (k + 5 < 64) ldm(k + 5);
      if (k + 3 < 64) mod(k + 3);
    });
    {
      const u32x4 v = wfrag<256>(L_PHI + ((l >> 4) & 1) * 512 + (4 * h + ((l & 15) >> 2)) * 32 + (l & 3) * 8);
#pragma unroll
      for (int i = 0; i < 4; i++) {
        ph0[i] = r < 16 ? v[i] : 0; ph1[i] = r < 16 ? 0 : v[i];
        int k0 = 8 * (i >> 1) + 4 * h + 2 * (i & 1);
        unsigned id = ((r & 15) == k0 ? 0x3f80u : 0) | ((r & 15) == k0 + 1 ? 0x3f800000u : 0);
        idp[i] = r < 16 ? id : 0; idm[i] = r < 16 ? 0 : id;
      }
    }
    TS(7);
    f32x16 pkk = mfma(ph1, pk8(ak, 8), mfma(ph0, pk8(ak, 0), Z));
    u32x4 v0 = pk8(av, 0), v1 = pk8(av, 8);
    const u32x4 ones = {0x3f803f80u, 0x3f803f80u, 0x3f803f80u, 0x3f803f80u};
    u32x4 pp, pm;
    phi(pkk, 0, pp, pm);
    f32x16 tr = mfma(pm, idm, mfma(pp, idp, Z));
    f32x16 kv0 = mfma(pk8(tr, 8), r < 16 ? v1 : ones, mfma(pk8(tr, 0), r < 16 ? v0 : ones, Z));
    phi(pkk, 8, pp, pm);
    tr = mfma(pm, idm, mfma(pp, idp, Z));
    f32x16 kv1 = mfma(pk8(tr, 8), r < 16 ? ones : v1, mfma(pk8(tr, 0), r < 16 ? ones : v0, Z));
#pragma unroll
    for (int i = 0; i < 4; i++) {
      f32x4 a, z;
#pragma unroll
      for (int q = 0; q < 4; q++) { a[q] = r < 16 ? kv0[4 * i + q] : kv1[4 * i + q]; z[q] = r < 16 ? kv1[4 * i + q] : kv0[4 * i + q]; }
      *(LAS f32x4*)(size_t)(R4 + w * 4352 + i * 1024 + l * 16) = a;
      if ((l & 15) == 0) *(LAS f32x4*)(size_t)(R4 + w * 4352 + 4096 + ((l >> 4) * 4 + i) * 16) = z;
    }
  }
  BAR();
  {
    f32x4 a = {0, 0, 0, 0}, z = {0, 0, 0, 0};
#pragma unroll
    for (int i = 0; i < 4; i++) { a += *(LAS f32x4*)(size_t)(R4 + i * 4352 + t * 16); z += *(LAS f32x4*)(size_t)(R4 + i * 4352 + 4096 + (t & 15) * 16); }
    const unsigned base = WS_KVP + (m1 * 16 + g1) * 4352;
    __builtin_amdgcn_raw_buffer_store_b128(u32x4{pk(a[0], a[1]), tag, pk(a[2], a[3]), tag}, rs, base + t * 16, 0, SC1);
    if (t < 16) __builtin_amdgcn_raw_buffer_store_b128(u32x4{pk(z[0], z[1]), tag, pk(z[2], z[3]), tag}, rs, base + 4096 + t * 16, 0, SC1);
  }
  TS(8);
  u32x4 ma = __builtin_amdgcn_raw_buffer_load_b128(rs, WS_MODG + 4096 + t * 16, 0, SC1), mc = __builtin_amdgcn_raw_buffer_load_b128(rs, WS_MODG + 8192 + t * 16, 0, SC1);
  u32x4 p[16], z;
  auto ldkv = [&](int i) { p[i] = __builtin_amdgcn_raw_buffer_load_b128(rs, t * 16, WS_KVP + (m1 * 16 + i) * 4352, SC1); };
  auto ldz = [&]() { z = __builtin_amdgcn_raw_buffer_load_b128(rs, (l & 15) * 4352 + 4096 + ((l >> 4) * 4 + w) * 16, WS_KVP + m1 * 16 * 4352, SC1); };
  FENCE();
  u32x4 qp0, qm0, qp1, qm1;
  {
    f32x16 aq = Z;
    gemm2<32, 1024, 256, 2>(R2 + trn, 0, af, aq, aq, NoFill());
    f32x16 pq = mfma(ph1, pk8(aq, 8), mfma(ph0, pk8(aq, 0), Z));
#pragma unroll
    for (int i = 0; i < 8; i++) ldkv(i);
    FENCE();
    phi(pq, 0, qp0, qm0);
    asm volatile("" : "+v"(qp0), "+v"(qm0) :: "memory");
#pragma unroll
    for (int i = 8; i < 16; i++) ldkv(i);
    ldz();
    FENCE();
    phi(pq, 8, qp1, qm1);
    asm volatile("" : "+v"(qp1), "+v"(qm1) :: "memory");
  }
  TS(9);
  {
    u32x4 a = ma, c = mc;
    while (!__all(a[1] == tag && a[3] == tag && c[1] == tag && c[3] == tag) && budget != 0) {
      budget--;
      FENCE();
      a = __builtin_amdgcn_raw_buffer_load_b128(rs, WS_MODG + 4096 + t * 16, 0, SC1);
      c = __builtin_amdgcn_raw_buffer_load_b128(rs, WS_MODG + 8192 + t * 16, 0, SC1);
    }
    if (t < 128) { c[0] = pk(1.f + bflo(c[0]), 1.f + bfhi(c[0])); c[2] = pk(1.f + bflo(c[2]), 1.f + bfhi(c[2])); }
    *(LAS u32x2*)(size_t)(L_MOD + t * 8) = u32x2{a[0], a[2]};
    *(LAS u32x2*)(size_t)(L_MOD + 2048 + t * 8) = u32x2{c[0], c[2]};
  }
  {
    f32x4 a = {0, 0, 0, 0};
    unsigned todo = 0xf;
    for (;;) {
#pragma unroll
      for (int q = 0; q < 4; q++)
        if (todo >> q & 1) {
          bool ok = true;
#pragma unroll
          for (int i = 4 * q; i < 4 * q + 4; i++) ok = ok && p[i][1] == tag && p[i][3] == tag;
          if (__all(ok)) {
            todo &= ~(1u << q);
            f32x4 c = {0, 0, 0, 0};
#pragma unroll
            for (int i = 4 * q; i < 4 * q + 4; i++) c += f32x4{bflo(p[i][0]), bfhi(p[i][0]), bflo(p[i][2]), bfhi(p[i][2])};
            p[4 * q] = __builtin_bit_cast(u32x4, c);
          }
        }
      if ((todo == 0 && __all(z[1] == tag && z[3] == tag)) || budget == 0) break;
      budget--;
      FENCE();
      ldz();
#pragma unroll
      for (int q = 0; q < 4; q++)
        if (todo >> q & 1) { ldkv(4 * q); ldkv(4 * q + 1); ldkv(4 * q + 2); ldkv(4 * q + 3); }
    }
    TS(10);
#pragma unroll
    for (int q = 0; q < 4; q++) { const u32x4 c = p[4 * q]; a += __builtin_bit_cast(f32x4, c); }
    *(LAS u32x2*)(size_t)(L_KVS + l * 32 + w * 8) = u32x2{pk(a[0], a[1]), pk(a[2], a[3])};
    f32x4 zz = {bflo(z[0]), bfhi(z[0]), bflo(z[2]), bfhi(z[2])};
#pragma unroll
    for (int i = 0; i < 4; i++) { zz[i] += dpp<0xB1>(zz[i]); zz[i] += dpp<0x4E>(zz[i]); zz[i] += dpp<0x124>(zz[i]); zz[i] += dpp<0x128>(zz[i]); }
    if ((l & 15) == 0) *(LAS u32x2*)(size_t)(L_ZS + (l >> 4) * 32 + w * 8) = u32x2{pk(zz[0], zz[1]), pk(zz[2], zz[3])};
  }
  u32x4 xres[2];
#pragma unroll
  for (int i = 0; i < 2; i++) xres[i] = __builtin_amdgcn_raw_buffer_load_b128(__builtin_amdgcn_make_buffer_rsrc((void*)x, 0, 2u << 20, 0x00020000),
      (l >> 2) * 1024 + (l & 3) * 16, (tile2 * 32 + i * 16) * 1024 + n0 * 2, 0);
  BAR();
  {
    u32x4 k0 = *(LAS u32x4*)(size_t)(L_KVS + l * 32), k1 = *(LAS u32x4*)(size_t)(L_KVS + l * 32 + 16);
    u32x4 z0 = *(LAS u32x4*)(size_t)(L_ZS + (l >> 4) * 32), z1 = *(LAS u32x4*)(size_t)(L_ZS + (l >> 4) * 32 + 16);
    f32x16 o0 = mfma(r < 16 ? k1 : z1, qm0, mfma(r < 16 ? k0 : z0, qp0, Z));
    f32x16 o1 = mfma(r < 16 ? z1 : k1, qm1, mfma(r < 16 ? z0 : k0, qp1, Z));
    float d0 = __builtin_amdgcn_rcpf(o0[8] + 1e-6f), d1 = __builtin_amdgcn_rcpf(o1[0] + 1e-6f);
    o0 *= d0; o1 *= d1;
    __builtin_amdgcn_raw_buffer_store_b128(pk8(o0, 0), rs, l * 16, WS_O + (tile * 32 + 2 * m1) * 1024, SC1);
    __builtin_amdgcn_raw_buffer_store_b128(pk8(o1, 8), rs, l * 16, WS_O + (tile * 32 + 2 * m1 + 1) * 1024, SC1);
  }
  VMCNT(0);
  set_flag(rs, FL_O + tile * 16 + m1, tag, l);
  TS(11);
  float xv[16], gav[16], cbv[16];
#pragma unroll
  for (int i = 0; i < 2; i++) *(LAS u32x4*)(size_t)(R4 + w * 4096 + (i * 16 + (l >> 2)) * 80 + (l & 3) * 16) = xres[i];
#pragma unroll
  for (int i = 0; i < 4; i++) {
    u32x2 xx = *(LAS u32x2*)(size_t)(R4 + w * 4096 + r * 80 + i * 16 + h * 8);
    u32x2 ga = *(LAS u32x2*)(size_t)(L_MOD + (n0 + 8 * i + 4 * h) * 2), cb = *(LAS u32x2*)(size_t)(L_MOD + 2048 + (n0 + 8 * i + 4 * h) * 2);
#pragma unroll
    for (int q = 0; q < 4; q++) {
      xv[4 * i + q] = q & 1 ? bfhi(xx[q >> 1]) : bflo(xx[q >> 1]); gav[4 * i + q] = q & 1 ? bfhi(ga[q >> 1]) : bflo(ga[q >> 1]);
      cbv[4 * i + q] = q & 1 ? bfhi(cb[q >> 1]) : bflo(cb[q >> 1]);
      asm volatile("" : "+v"(xv[4 * i + q]), "+v"(gav[4 * i + q]), "+v"(cbv[4 * i + q]));
    }
  }
  BAR();
  unsigned x1[8];
  wait_flags(rs, FL_O + tile2 * 16 + kh * 8, 8, tag, l, budget);
  TS(12);
  {
#pragma unroll
    for (int s = 0; s < PRE; s++) af[s] = LDF(WS_O + (tile2 * 32 + kh * 16 + s) * 1024);
    f32x16 a0 = Z, a1 = Z;
    const unsigned b0 = trp + kh * (16384 + 32768), b1 = trp + kh * 16384 + (1 - kh) * 32768;
    gemm2<16, 1024, 512, 0>(b1, b0, af, a1, a0, [&](int k) { if (k + PRE < 16) af[k + PRE] = LDF(WS_O + (tile2 * 32 + kh * 16 + k + PRE) * 1024); });
#pragma unroll
    for (int i = 0; i < 4; i++) *(LAS f32x4*)(size_t)(R4 + (w ^ 2) * 4096 + i * 1024 + l * 16) = f32x4{a1[4 * i], a1[4 * i + 1], a1[4 * i + 2], a1[4 * i + 3]};
    TS(13);
    BAR();
    float s1 = 0, s2 = 0; f32x16 xs;
#pragma unroll
    for (int i = 0; i < 4; i++) {
      f32x4 o = *(LAS f32x4*)(size_t)(R4 + w * 4096 + i * 1024 + l * 16);
#pragma unroll
      for (int q = 0; q < 2; q++) {
        const int e = 4 * i + 2 * q;
        const unsigned v = pk(fmaf(gav[e], a0[e] + o[2 * q], xv[e]), fmaf(gav[e + 1], a0[e + 1] + o[2 * q + 1], xv[e + 1]));
        const float v0 = bflo(v), v1 = bfhi(v);
        x1[e >> 1] = v; s1 += v0 + v1; s2 = fmaf(v0, v0, fmaf(v1, v1, s2)); xs[e] = v0 * cbv[e]; xs[e + 1] = v1 * cbv[e + 1];
      }
    }
    __builtin_amdgcn_raw_buffer_store_b128(pk8(xs, 0), rs, l * 16, WS_XS + (tile2 * 32 + (n0 >> 4)) * 1024, SC1);
    __builtin_amdgcn_raw_buffer_store_b128(pk8(xs, 8), rs, l * 16, WS_XS + (tile2 * 32 + (n0 >> 4) + 1) * 1024, SC1);
    s1 = sum32(s1); s2 = sum32(s2);
    if (l < 32) __builtin_amdgcn_raw_buffer_store_b64(u32x2{__float_as_uint(s1), __float_as_uint(s2)}, rs, l * 8, WS_ST2 + (tile2 * 16 + (n0 >> 5)) * 256, SC1);
    VMCNT(0);
    set_flag(rs, FL_X + tile2 * 16 + (n0 >> 5), tag, l);
    if (b == 0 && t == 0) __builtin_amdgcn_raw_buffer_store_b32(tag, rs, WS_EPOCH, 0, SC1);
  }
  TS(14);
  if (w < 3 && (three || w < 2)) {
    u32x4 a;
    FENCE();
    for (;;) {
      a = __builtin_amdgcn_raw_buffer_load_b128(rs, WS_UVG + (h * 1376 + du_of(m3, w) * 32 + r) * 16, 0, SC1);
      if (__all(a[1] == tag && a[3] == tag) || budget == 0) break;
      budget--;
      FENCE();
    }
    const float sc = h ? 1.f : -1.44269504088896f;
    *(LAS f32x2*)(size_t)(L_UV + (w * 32 + r) * 16 + h * 8) = f32x2{__uint_as_float(a[0]) * sc, __uint_as_float(a[2]) * sc};
  }
  wait_flags(rs, FL_X + tile3 * 16, 16, tag, l, budget);
  TS(15);
  float mean2, rstd2;
  f32x16 ga0 = Z, u0 = Z, ga1 = Z, u1 = Z;
  {
    u32x2 st[8];
#pragma unroll
    for (int s = 0; s < PRE; s++) af[s] = LDF(WS_XS + (tile3 * 32 + s) * 1024);
    FENCE();
    gemm2<32, 1024, 512, 0>(R3 + trp, R2 + trp, af, ga0, u0, [&](int k) {
      if (!(k & 1) && (k >> 1) + PRE < 32) af[(k >> 1) + PRE] = LDF(WS_XS + (tile3 * 32 + (k >> 1) + PRE) * 1024);
      if (k >= 40 && k < 48) st[k - 40] = __builtin_amdgcn_raw_buffer_load_b64(rs, (h * 8 + k - 40) * 256 + r * 8, WS_ST2 + tile3 * 16 * 256, SC1);
    });
    float s1 = 0, s2 = 0;
#pragma unroll
    for (int i = 0; i < 8; i++) { s1 += __uint_as_float(st[i][0]); s2 += __uint_as_float(st[i][1]); }
    mean2 = sum32(s1) * (1.f / 512);
    rstd2 = __builtin_amdgcn_rsqf(fmaxf(sum32(s2) * (1.f / 512) - mean2 * mean2, 0.f) + 1e-5f);
  }
  const float nm = -mean2 * rstd2, rl = -1.44269504088896f * rstd2;
  const unsigned uvb = opq(L_UV + 64 * h);
  f32x4 uv[2]; float eg, eu; unsigned hv[8]; float hlo;
  auto ep = [&](int k, int p, const f32x16& ag, const f32x16& au, bool pin) {
    if (k >= 51) return;
    if (k % 3 == 0 && k < 48) uv[(k / 3) & 1] = *(LAS f32x4*)(size_t)(uvb + (p * 32 + 8 * (k / 12) + (k / 3 & 3)) * 16);
    if (k < 3) return;
    const int e = k / 3 - 1, c = k % 3;
    const f32x4 d = uv[e & 1];
    if (c == 0) { eg = __builtin_amdgcn_exp2f(fmaf(ag[e], rl, fmaf(nm, d[0], d[1]))); if (pin) asm volatile("" : "+v"(eg)); }
    if (c == 1) { eg = __builtin_amdgcn_rcpf(1.f + eg); eu = fmaf(nm, d[2], d[3]); if (pin) asm volatile("" : "+v"(eg), "+v"(eu)); }
    if (c == 2) {
      float v = fmaf(au[e], rstd2, eu) * eg;
      if (e & 1) hv[e >> 1] = pin ? opq(pk(hlo, v)) : pk(hlo, v); else { hlo = v; if (pin) asm volatile("" : "+v"(hlo)); }
      if ((e & 7) == 7) __builtin_amdgcn_raw_buffer_store_b128(u32x4{hv[(e >> 1) - 3], hv[(e >> 1) - 2], hv[(e >> 1) - 1], hv[e >> 1]}, rs, l * 16,
                                                               WS_HID + (tile3 * 86 + du_of(m3, p) * 2 + (e >> 3)) * 1024, SC1);
    }
  };
  {
    TS(16);
    BAR();
    gemm2<32, 1024, 512, 0>(R0 + trp, R1 + trp, af, ga1, u1, [&](int k) { ep(k, 0, ga0, u0, true); });
    TS(17);
    BAR();
    const unsigned hb = WS_HID + (tile2 * 86 + kh) * 1024;
    const unsigned fd = WS_FLG + (FL_D + tile2 * 48 + (l & 15)) * 64;
    unsigned fv = 0;
    if (three) {
      ga0 = Z; u0 = Z;
      gemm2<32, 1024, 512, 0>(R3 + trp, R2 + trp, af, ga0, u0, [&](int k) {
        if (k == 10) { VMCNT(0); set_flag(rs, FL_D + tile3 * 48 + m3, tag, l); }
        ep(k, 1, ga1, u1, true);
        if (k == 56) { fv = __builtin_amdgcn_raw_buffer_load_b32(rs, fd, 0, SC1); FENCE(); }
      });
      ga1 = ga0; u1 = u0;
    } else { VMCNT(0); set_flag(rs, FL_D + tile3 * 48 + m3, tag, l); }
    TS(18);
    BAR();
    const int pl = three ? 2 : 1;
#pragma unroll
    for (int k = 0; k < 24; k++) ep(k, pl, ga1, u1, false);
    asm volatile("" : "+v"(hv[0]), "+v"(hv[1]), "+v"(hv[2]));
    VMCNT(0);
    if (three) set_flag(rs, FL_D + tile3 * 48 + 16 + m3, tag, l);
    const bool early = three && __all(fv == tag);
    if (early) {
#pragma unroll
      for (int s = 0; s < 8; s++) af[s] = LDF(hb + du_of(s, 0) * 2048);
    }
    FENCE();
#pragma unroll
    for (int k = 24; k < 64; k++) ep(k, pl, ga1, u1, false);
    FENCE();
    if (!early) {
      if (!three) { VMCNT(0); set_flag(rs, FL_D + tile3 * 48 + 16 + m3, tag, l); }
      wait_flags(rs, FL_D + tile2 * 48, 16, tag, l, budget);
#pragma unroll
      for (int s = 0; s < 8; s++) af[s] = LDF(hb + du_of(s, 0) * 2048);
    }
    TS(19);
    f32x16 a0 = Z, a1 = Z;
    const unsigned b0 = trp + kh * 2048 + kh * 512, b1 = trp + kh * 2048 + (1 - kh) * 512;
    gemm2<16, 4096, 1024, 0>(R0 + b1, R0 + b0, af, a1, a0, [&](int k) {
      if (k < 8) af[8 + k] = LDF(hb + du_of(8 + k, 0) * 2048);
      if (k == KF) {
        VMCNT(0);
        if (three) set_flag(rs, FL_D + tile3 * 48 + 32 + m3, tag, l);
        fv = __builtin_amdgcn_raw_buffer_load_b32(rs, fd + 16 * 64, 0, SC1); FENCE();
      }
    });
    TS(20);
    if (!__all(fv == tag)) wait_flags(rs, FL_D + tile2 * 48 + 16, 16, tag, l, budget);
    FENCE();
#pragma unroll
    for (int s = 0; s < 12; s++) af[16 + s] = LDF(hb + du_of(s, 1) * 2048);
    BAR();
    TS(21);
    gemm2<16, 4096, 1024, 0>(R2 + b1, R2 + b0, af + 16, a1, a0, [&](int k) {
      if (k < 4) af[28 + k] = LDF(hb + du_of(12 + k, 1) * 2048);
      if (k == 4) { fv = __builtin_amdgcn_raw_buffer_load_b32(rs, WS_FLG + (FL_D + tile2 * 48 + 32 + (l < 11 ? l : 0)) * 64, 0, SC1); FENCE(); }
      if (k == 20) { if (!__all(fv == tag)) wait_flags(rs, FL_D + tile2 * 48 + 32, 11, tag, l, budget); FENCE(); }
      if (k >= 20 && k < 31) af[k - 20] = LDF(hb + du_of(k - 20, 2) * 2048);
    });
    TS(22);
    BAR();
    gemm2<6, 4096, 1024, 0>(R4 + b1, R4 + b0, af, a1, a0, NoFill());
    gemm2<5, 4096, 1024, 0>(R0 + b1, R0 + b0, af + 6, a1, a0, NoFill());
    const int l2 = opq(l);
#pragma unroll
    for (int i = 0; i < 4; i++) *(LAS f32x4*)(size_t)(R2 + (w ^ 2) * 4096 + i * 1024 + l2 * 16) = f32x4{a1[4 * i], a1[4 * i + 1], a1[4 * i + 2], a1[4 * i + 3]};
    TS(23);
    BAR();
#pragma unroll
    for (int i = 0; i < 4; i++) {
      f32x4 o = *(LAS f32x4*)(size_t)(R2 + w * 4096 + i * 1024 + l2 * 16);
      u32x2 gb = *(LAS u32x2*)(size_t)(L_MOD + 3072 + (n0 + 8 * i + 4 * (l2 >> 5)) * 2);
      u32x2 y = {pk(fmaf(bflo(gb[0]), a0[4 * i] + o[0], bflo(x1[2 * i])), fmaf(bfhi(gb[0]), a0[4 * i + 1] + o[1], bfhi(x1[2 * i]))),
                 pk(fmaf(bflo(gb[1]), a0[4 * i + 2] + o[2], bflo(x1[2 * i + 1])), fmaf(bfhi(gb[1]), a0[4 * i + 3] + o[3], bfhi(x1[2 * i + 1])))};
      *(LAS u32x2*)(size_t)(R3 + w * 4096 + (l2 & 31) * 80 + i * 16 + (l2 >> 5) * 8) = y;
    }
#pragma unroll
    for (int i = 0; i < 2; i++)
      __builtin_amdgcn_raw_buffer_store_b128(*(LAS u32x4*)(size_t)(R3 + w * 4096 + (i * 16 + (l2 >> 2)) * 80 + (l2 & 3) * 16), __builtin_amdgcn_make_buffer_rsrc(out, 0, 2u << 20, 0x00020000),
                                             (l2 >> 2) * 1024 + (l2 & 3) * 16, (tile2 * 32 + i * 16) * 1024 + n0 * 2, SC1);
  }
  TS(24); TS(25);
}

extern "C" void kernel_launch(void* const* d_in, const int* in_sizes, int n_in, void* d_out, int out_size, void* d_ws, size_t ws_size,
                              hipStream_t stream) {
  (void)hipFuncSetAttribute((const void*)fused_block, hipFuncAttributeMaxDynamicSharedMemorySize, 163840);
  fused_block<<<256, 320, 163840, stream>>>((const u16*)d_in[0], (const u16*)d_in[1], (const u16*)d_in[2], (const u16*)d_in[3], (const u16*)d_in[5],
                                            (const u16*)d_in[6], (const u16*)d_in[7], (const u16*)d_in[8], (const u16*)d_in[9], (u16*)d_out, d_ws);
}
```
